```python
import torch

def _softmax(x, axis=-1):
    return torch.softmax(x, dim=axis)
def _gelu(x):
    return torch.nn.functional.gelu(x, approximate="tanh")
def _silu(x):
    return torch.nn.functional.silu(x)
def _sigmoid(x):
    return torch.sigmoid(x)
def _layernorm(x, axis=-1, eps=1e-5):
    m = x.mean(dim=axis, keepdim=True)
    v = x.var(dim=axis, keepdim=True, unbiased=False)
    return (x - m) / torch.sqrt(v + eps)
def _scatter_add(tgt, idx, src, axis=0):
    return tgt.clone().index_add_(axis, idx, src)
def _segment_sum(src, seg, n):
    out = torch.zeros((n,) + src.shape[1:], dtype=src.dtype)
    return out.index_add_(0, seg, src)
def _pad(x, pw):
    flat = tuple(v for pair in reversed(pw) for v in pair)
    return torch.nn.functional.pad(x, flat)

kernel_name = 'sequential_preLN__sliding_local_dilated__swiglu_ffn__prompt'

def setup_inputs():

    _w = (lambda a: (lambda t: t.to(torch.bfloat16) if t.is_floating_point() else t)(torch.as_tensor(a)))
    def _A_setup():
        B, S, D = 1, 2048, 640
        return {
            "x": _w((lambda *s: torch.randn(*s, dtype=torch.bfloat16) * 0.05)(B, S, D)),
            "wqkv": _w((lambda *s: torch.randn(*s, dtype=torch.bfloat16) * 0.05)(D, 3 * D)),
            "wo": _w((lambda *s: torch.randn(*s, dtype=torch.bfloat16) * 0.05)(D, D)),
        }
    _A_in = _A_setup()

    def _B_setup():
        B, S, D, F = 1, 2048, 640, 1728
        return {
            "x": _w((lambda *s: torch.randn(*s, dtype=torch.bfloat16) * 0.05)(B, S, D)),
            "w_gate": _w((lambda *s: torch.randn(*s, dtype=torch.bfloat16) * 0.05)(D, F)),
            "w_up": _w((lambda *s: torch.randn(*s, dtype=torch.bfloat16) * 0.05)(D, F)),
            "w_down": _w((lambda *s: torch.randn(*s, dtype=torch.bfloat16) * 0.05)(F, D)),
        }
    _B_in = _B_setup()

    return {
        "A_x": _A_in["x"],
        "A_wqkv": _A_in["wqkv"],
        "A_wo": _A_in["wo"],
        "B_x": _B_in["x"],
        "B_w_gate": _B_in["w_gate"],
        "B_w_up": _B_in["w_up"],
        "B_w_down": _B_in["w_down"],
    }

def reference(A_x, A_wqkv, A_wo, B_x, B_w_gate, B_w_up, B_w_down):
    def _A_ref(x, wqkv, wo):
        B, S, D = x.shape
        H = 32; Dh = D // H
        W, dil = 8, 2
        qkv = (lambda x, *d: x.permute(*d))((x @ wqkv).reshape(B, S, 3, H, Dh), 2, 0, 3, 1, 4)
        q, k, v = qkv[0], qkv[1], qkv[2]
        scale = Dh ** -0.5
        pos = torch.arange(S)
        off = torch.arange(W) * dil
        idx = pos[:, None] - off[None, :]
        mask = idx >= 0
        idx_c = torch.where(mask, idx, 0).reshape(-1)
        kg = k[:, :, idx_c, :].reshape(B, H, S, W, Dh)
        vg = v[:, :, idx_c, :].reshape(B, H, S, W, Dh)
        logits = torch.einsum("bhsd,bhswd->bhsw", q, kg) * scale
        logits = torch.where(mask[None, None, :, :], logits, -1e9)
        attn = _softmax(logits, axis=-1)
        o = torch.einsum("bhsw,bhswd->bhsd", attn, vg)
        return (lambda x, *d: x.permute(*d))(o, 0, 2, 1, 3).reshape(B, S, D) @ wo

    def _B_ref(x, w_gate, w_up, w_down):
        g = _silu(x @ w_gate)
        u = x @ w_up
        return (g * u) @ w_down

    x = A_x
    h = x + _A_ref(_layernorm(x, axis=-1), A_wqkv, A_wo)
    h2 = h + _B_ref(_layernorm(h, axis=-1), B_w_gate, B_w_up, B_w_down)
    return h2
```

```cpp
#include <hip/hip_runtime.h>
#include <hip/hip_bf16.h>
#include <stdint.h>

typedef __attribute__((ext_vector_type(8))) __bf16 bf16x8;
typedef __attribute__((ext_vector_type(2))) __bf16 bf16x2;
typedef __attribute__((ext_vector_type(4))) float f32x4;
typedef __attribute__((ext_vector_type(2))) float f32x2;
typedef __attribute__((ext_vector_type(4))) unsigned u32x4;
typedef __attribute__((ext_vector_type(2))) unsigned u32x2;
typedef unsigned long long u64;
typedef __attribute__((address_space(1))) u64 gu64;
typedef __attribute__((address_space(3))) u32x4 lds_u32x4;
typedef __attribute__((address_space(3))) u32x2 lds_u32x2;
typedef __attribute__((address_space(3))) f32x4 lds_f32x4;
typedef __attribute__((address_space(3))) f32x2 lds_f32x2;
typedef __attribute__((address_space(3))) char lds_char;

constexpr int S = 2048, D = 640, F = 1728;
constexpr unsigned WS_EPOCH = 0;
constexpr unsigned WS_FLAGW = 4096;
constexpr unsigned WS_FLAG = 32768;
constexpr unsigned WS_PROF = 98304;
constexpr unsigned WS_W1 = 1u << 18;
constexpr unsigned WS_W2 = WS_W1 + 16 * 160 * 1024;
constexpr unsigned WS_W4 = WS_W2 + 8 * 100 * 1024;
constexpr unsigned WS_W3 = WS_W4 + 8 * 270 * 1024;
constexpr unsigned WS_O = WS_W3 + 108 * 40 * 1024;
constexpr unsigned WS_H = WS_O + 128 * 20 * 1024;
constexpr unsigned WS_A = WS_H + 128 * 20 * 1024;
constexpr unsigned WS_END = WS_A + 128 * 54 * 1024;
constexpr unsigned SPIN_CAP = 1u << 22;

#define RLX_AGENT __ATOMIC_RELAXED, __HIP_MEMORY_SCOPE_AGENT
#define BAR() asm volatile("s_waitcnt lgkmcnt(0)\n\ts_barrier" ::: "memory")
#define DRAIN() asm volatile("s_waitcnt vmcnt(0)" ::: "memory")
#define CFENCE() do { asm volatile("" ::: "memory"); __builtin_amdgcn_sched_barrier(0); } while (0)
#define MFMA(a, b, c) __builtin_amdgcn_mfma_f32_16x16x32_bf16(__builtin_bit_cast(bf16x8, a), __builtin_bit_cast(bf16x8, b), c, 0, 0, 0)
#define STAMPC(i)
#define STAMP(i)

__device__ __forceinline__ unsigned pk_bf16(float a, float b) {
  f32x2 v = {a, b};
  return __builtin_bit_cast(unsigned, __builtin_convertvector(v, bf16x2));
}
__device__ __forceinline__ float bf_lo(unsigned u) { return __builtin_bit_cast(float, u << 16); }
__device__ __forceinline__ float bf_hi(unsigned u) { return __builtin_bit_cast(float, u & 0xffff0000u); }
__device__ __forceinline__ float dot2(unsigned a, unsigned b, float c) {
  return __builtin_amdgcn_fdot2_f32_bf16(__builtin_bit_cast(bf16x2, a), __builtin_bit_cast(bf16x2, b), c, false);
}
template <int CTRL> __device__ __forceinline__ float dpp(float v) {
  return __builtin_bit_cast(float, __builtin_amdgcn_update_dpp(0, __builtin_bit_cast(int, v), CTRL, 0xf, 0xf, true));
}
__device__ __forceinline__ float sum16(float v) {
  v += dpp<0xB1>(v);
  v += dpp<0x4E>(v);
  v += dpp<0x141>(v);
  v += dpp<0x140>(v);
  return v;
}
typedef __amdgpu_buffer_rsrc_t rsrc_t;
__device__ __forceinline__ u32x4 ld16(rsrc_t r, unsigned voff, unsigned soff) {
  return __builtin_amdgcn_raw_buffer_load_b128(r, voff, soff, 16);
}
__device__ __forceinline__ void st16(rsrc_t r, u32x4 v, unsigned voff, unsigned soff) {
  __builtin_amdgcn_raw_buffer_store_b128(v, r, voff + soff, 0, 16);
}
__device__ __forceinline__ void st8(rsrc_t r, u32x2 v, unsigned voff, unsigned soff) {
  __builtin_amdgcn_raw_buffer_store_b64(v, r, voff, soff, 16);
}
__device__ __forceinline__ void wait_group(char* ws, int phase, int g, int lane, u64 tag) {
  gu64* f = (gu64*)(ws + WS_FLAG + (unsigned)(phase * 256 + g * 16 + (lane & 15)) * 64u);
  for (unsigned spins = 0; spins < SPIN_CAP; ++spins) {
    u64 v = __hip_atomic_load(f, RLX_AGENT);
    if (__all(v == tag)) break;
    __builtin_amdgcn_s_sleep(1);
  }
  CFENCE();
}
__device__ __forceinline__ void publish(char* ws, int phase, int g, int j, u64 tag) {
  DRAIN();
  BAR();
  if (threadIdx.x == 0) __hip_atomic_store((gu64*)(ws + WS_FLAG + (unsigned)(phase * 256 + g * 16 + j) * 64u), tag, RLX_AGENT);
}

__global__ void __launch_bounds__(512, 2)
fused_block(const unsigned short* __restrict__ x, const unsigned short* __restrict__ wqkv,
            const unsigned short* __restrict__ wo, const unsigned short* __restrict__ wg,
            const unsigned short* __restrict__ wu, const unsigned short* __restrict__ wd,
            unsigned short* __restrict__ out, char* ws) {
  __shared__ __attribute__((aligned(16))) char smem_[163840];
  lds_char* smem = (lds_char*)smem_;
  const int tid = threadIdx.x, lane = tid & 63;
  const int w = __builtin_amdgcn_readfirstlane(tid >> 6);
  const int b = blockIdx.x, xcd = b & 7, slot = b >> 3;
  const int g = 4 * (xcd >> 1) + (slot >> 3), j = 8 * (xcd & 1) + (slot & 7), p = j >> 1;
  const int li = lane & 15, lq = lane >> 4;
  const rsrc_t rs = __builtin_amdgcn_make_buffer_rsrc(ws, 0, WS_END, 0x00020000);
  const unsigned lane16 = lane * 16;
  STAMP(0); STAMPC(14);
  const u64 tag = __hip_atomic_load((gu64*)(ws + WS_EPOCH), RLX_AGENT) + 1;

  const int ftbase = 27 * (p >> 1) + 14 * (p & 1), nft = 14 - (p & 1);
  unsigned short rv[5][8];
  unsigned rdst[5];
  bool rzero[5];
#pragma unroll
  for (int r = 0; r < 5; ++r) {
    int f = r * 256 + (g * 2 + (j & 1)) * 8 + w;
    if (f >= 690 + 40 * nft) f = w;
    const unsigned short* src; int stride, row, col, jump = 0; bool z = false;
    if (f < 320) {
      int ks = f >> 4, jj = 2 * p + ((f >> 3) & 1), t = f & 7, c = 16 * t + li;
      z = c >= 120; int part = c / 40; col = z ? 0 : part * 640 + 40 * jj + (c - part * 40);
      src = wqkv; stride = 1920; row = 32 * ks + 8 * lq; rdst[r] = WS_W1 + ((jj * 8 + t) * 20 + ks) * 1024;
    } else if (f < 420) {
      int f2 = f - 320, ks = f2 / 5, t = f2 - ks * 5, O = 4 * ks + lq, jj = O / 5, o = O - jj * 5;
      row = 40 * jj + (o < 4 ? 20 * (o >> 1) + 8 * (o & 1) : 16); jump = o < 4 ? 0 : 16;
      src = wo; stride = 640; col = 80 * p + 16 * t + li; rdst[r] = WS_W2 + (p * 100 + f2) * 1024;
    } else if (f < 690) {
      int f2 = f - 420, ks = f2 / 5, t = f2 - ks * 5;
      src = wd; stride = 640; row = 32 * ks + 8 * lq; col = 80 * p + 16 * t + li; rdst[r] = WS_W4 + (p * 270 + f2) * 1024;
    } else {
      int f2 = f - 690, ks = f2 / (2 * nft), rem = f2 - ks * 2 * nft, m = rem >= nft, ft = ftbase + rem - m * nft;
      src = m ? wu : wg; stride = F; row = 32 * ks + 8 * lq; col = 16 * ft + li; rdst[r] = WS_W3 + ((ft * 2 + m) * 20 + ks) * 1024;
    }
    rzero[r] = z;
    const unsigned short* s0 = src + row * stride + col;
#pragma unroll
    for (int e = 0; e < 8; ++e) rv[r][e] = s0[(e + (e >= 4 ? jump : 0)) * stride];
  }
  CFENCE();
  u32x4 xr[5][5];
#pragma unroll
  for (int i = 0; i < 5; ++i) {
    int row = i < 4 ? 128 * g + 4 * (w + 8 * i) + lq : 128 * g - 16 + 4 * (w & 3) + lq;
    row = row < 0 ? 0 : row;
    const u32x4* xp = (const u32x4*)(x + row * D) + li;
    if (i < 4 || w < 4) {
#pragma unroll
      for (int c = 0; c < 5; ++c) xr[i][c] = xp[16 * c];
    }
  }
  CFENCE();
#pragma unroll
  for (int r = 0; r < 5; ++r) {
    u32x4 v;
#pragma unroll
    for (int e = 0; e < 4; ++e) v[e] = rzero[r] ? 0u : ((unsigned)rv[r][2 * e] | ((unsigned)rv[r][2 * e + 1] << 16));
    st16(rs, v, lane16, __builtin_amdgcn_readfirstlane(rdst[r]));
  }
  DRAIN();
  if (lane == 0) __hip_atomic_store((gu64*)(ws + WS_FLAGW + (unsigned)((p * 32 + g * 2 + (j & 1)) * 8 + w) * 8u), tag, RLX_AGENT);
  STAMP(1);
#pragma unroll
  for (int i = 0; i < 5; ++i) {
    if (i < 4 || w < 4) {
      float v[40], s = 0.f;
#pragma unroll
      for (int c = 0; c < 5; ++c)
#pragma unroll
        for (int e = 0; e < 4; ++e) { v[c * 8 + 2 * e] = bf_lo(xr[i][c][e]); v[c * 8 + 2 * e + 1] = bf_hi(xr[i][c][e]); }
#pragma unroll
      for (int e = 0; e < 40; ++e) s += v[e];
      const float mean = sum16(s) * (1.f / D);
      float q = 0.f;
#pragma unroll
      for (int e = 0; e < 40; ++e) { v[e] -= mean; q += v[e] * v[e]; }
      const float rstd = rsqrtf(sum16(q) * (1.f / D) + 1e-5f);
#pragma unroll
      for (int c = 0; c < 5; ++c)
#pragma unroll
        for (int e = 0; e < 4; ++e) xr[i][c][e] = pk_bf16(v[c * 8 + 2 * e] * rstd, v[c * 8 + 2 * e + 1] * rstd);
      if (i < 4) {
        const int rl = 4 * (w + 8 * i) + lq;
#pragma unroll
        for (int c = 0; c < 5; ++c)
          *(lds_u32x4*)(smem + (((rl >> 4) * 20 + 4 * c + (li >> 2)) * 64 + (li & 3) * 16 + ((rl & 15) ^ (li & 7))) * 16) = xr[i][c];
      }
    }
  }
  if (w == 7) {
    for (unsigned spins = 0; spins < SPIN_CAP; ++spins) {
      u32x4 a = ld16(rs, lane16, WS_FLAGW + p * 2048), c = ld16(rs, lane16, WS_FLAGW + p * 2048 + 1024);
      const unsigned tl = (unsigned)tag, th = (unsigned)(tag >> 32);
      bool ok = a[0] == tl && a[1] == th && a[2] == tl && a[3] == th && c[0] == tl && c[1] == th && c[2] == tl && c[3] == th;
      if (__all(ok)) break;
      __builtin_amdgcn_s_sleep(1);
      CFENCE();
    }
  }
  BAR();
  STAMP(2);
  u32x4 w1[20], w2[15], w3[2][20];
  const int ks2 = w < 4 ? 3 * w : 12 + 2 * (w - 4);
  const int ft = ftbase + 7 * (j & 1) + w;
  const bool act3 = w < 7 && 7 * (j & 1) + w < nft;
  const unsigned w3off = WS_W3 + (act3 ? ft : ftbase) * 40 * 1024;
#pragma unroll
  for (int k = 0; k < 20; ++k) w1[k] = ld16(rs, lane16, WS_W1 + ((j * 8 + w) * 20 + k) * 1024);
#pragma unroll
  for (int k = 0; k < 15; ++k) w2[k] = ld16(rs, lane16, WS_W2 + (p * 100 + ks2 * 5 + ((k < 10 || w < 4) ? k : 0)) * 1024);
  CFENCE();
  {
    f32x4 acc[8];
#pragma unroll
    for (int r = 0; r < 8; ++r) acc[r] = f32x4{0.f, 0.f, 0.f, 0.f};
    const unsigned ra0 = (lq * 16 + (li ^ lq)) * 16, ra1 = (lq * 16 + (li ^ (lq + 4))) * 16;
#pragma unroll
    for (int k = 0; k < 20; ++k) {
#pragma unroll
      for (int r = 0; r < 8; ++r) {
        u32x4 a = *(lds_u32x4*)(smem + ((k & 1) ? ra1 : ra0) + (r * 20 + k) * 1024);
        acc[r] = MFMA(w1[k], a, acc[r]);
      }
    }
    BAR();
    if (w < 4) {
      const int rl = 4 * w + lq;
#pragma unroll
      for (int c = 0; c < 5; ++c)
        *(lds_u32x4*)(smem + ((4 * c + (li >> 2)) * 64 + (li & 3) * 16 + (rl ^ (li & 7))) * 16) = xr[4][c];
    }
#pragma unroll
    for (int r = 0; r < 8; ++r)
      *(lds_u32x2*)(smem + 20480 + (16 + 16 * r + li) * 272 + (4 * w + lq) * 8) =
          u32x2{pk_bf16(acc[r][0], acc[r][1]), pk_bf16(acc[r][2], acc[r][3])};
    BAR();
    f32x4 ah = {0.f, 0.f, 0.f, 0.f};
#pragma unroll
    for (int k = 0; k < 20; ++k) {
      u32x4 a = *(lds_u32x4*)(smem + ((k & 1) ? ra1 : ra0) + k * 1024);
      ah = MFMA(w1[k], a, ah);
    }
    *(lds_u32x2*)(smem + 20480 + li * 272 + (4 * w + lq) * 8) = u32x2{pk_bf16(ah[0], ah[1]), pk_bf16(ah[2], ah[3])};
  }
#pragma unroll
  for (int k = 0; k < 12; ++k) { w3[0][k] = ld16(rs, lane16, w3off + k * 1024); w3[1][k] = ld16(rs, lane16, w3off + (20 + k) * 1024); }
  BAR();
  STAMP(3);
  {
    const int tok = tid >> 2, hd = (tid >> 1) & 1, hf = tid & 1;
    lds_char* base = smem + 20480 + (16 + tok) * 272 + hd * 40;
    u32x2 qv[5];
#pragma unroll
    for (int c = 0; c < 5; ++c) qv[c] = *(lds_u32x2*)(base + c * 8);
    lds_char* kb = base - hf * 8 * 272 - 6 * 272;
    float lg[4];
#pragma unroll
    for (int i = 0; i < 4; ++i) {
      float s = 0.f;
#pragma unroll
      for (int c = 0; c < 5; ++c) {
        u32x2 kv = *(lds_u32x2*)(kb + (6 - 2 * i) * 272 + 80 + c * 8);
        s = dot2(qv[c][0], kv[0], s); s = dot2(qv[c][1], kv[1], s);
      }
      lg[i] = (128 * g + tok - 8 * hf - 2 * i >= 0) ? s * 0.22360679774997896f : -1e9f;
    }
    float mx = fmaxf(fmaxf(lg[0], lg[1]), fmaxf(lg[2], lg[3]));
    mx = fmaxf(mx, dpp<0xB1>(mx));
    float sum = 0.f;
#pragma unroll
    for (int i = 0; i < 4; ++i) { lg[i] = __expf(lg[i] - mx); sum += lg[i]; }
    sum += dpp<0xB1>(sum);
    const float inv = __builtin_amdgcn_rcpf(sum);
    float o[20];
#pragma unroll
    for (int d = 0; d < 20; ++d) o[d] = 0.f;
#pragma unroll
    for (int i = 0; i < 4; ++i) {
      const float pr = lg[i] * inv;
#pragma unroll
      for (int c = 0; c < 5; ++c) {
        u32x2 vv = *(lds_u32x2*)(kb + (6 - 2 * i) * 272 + 160 + c * 8);
        o[4 * c] += pr * bf_lo(vv[0]); o[4 * c + 1] += pr * bf_hi(vv[0]);
        o[4 * c + 2] += pr * bf_lo(vv[1]); o[4 * c + 3] += pr * bf_hi(vv[1]);
      }
    }
#pragma unroll
    for (int d = 0; d < 20; ++d) o[d] += dpp<0xB1>(o[d]);
    u32x4 v;
#pragma unroll
    for (int e = 0; e < 4; ++e) v[e] = hf ? pk_bf16(o[8 + 2 * e], o[9 + 2 * e]) : pk_bf16(o[2 * e], o[2 * e + 1]);
    const unsigned ob = WS_O + (8 * g + w) * 20 * 1024;
    st16(rs, v, (5 * j + 2 * hd + hf) * 256 + (tok & 15) * 16, ob);
    if (!hf) st8(rs, u32x2{pk_bf16(o[16], o[17]), pk_bf16(o[18], o[19])}, (5 * j + 4) * 256 + (tok & 15) * 16 + hd * 8, ob);
  }
  const int rt0 = 8 * g + 4 * (j & 1);
  u32x2 res[3];
#pragma unroll
  for (int n = 0; n < 3; ++n) {
    const int u = (n < 2 || w < 4) ? w + 8 * n : w, rt = u / 5, t = u - 5 * rt;
    res[n] = *(const u32x2*)(x + ((rt0 + rt) * 16 + li) * D + 80 * p + 16 * t + 4 * lq);
  }
  publish(ws, 0, g, j, tag);
  STAMP(4);
  if (w == 7) wait_group(ws, 0, g, lane, tag);
  BAR();
  STAMP(5);
  {
    u32x4 a[4][3];
#pragma unroll
    for (int r = 0; r < 4; ++r)
#pragma unroll
      for (int k = 0; k < 3; ++k) a[r][k] = ld16(rs, lane16, WS_O + ((rt0 + r) * 20 + ks2 + ((k < 2 || w < 4) ? k : 0)) * 1024);
#pragma unroll
    for (int r = 0; r < 4; ++r) {
      f32x4 acc[5];
#pragma unroll
      for (int t = 0; t < 5; ++t) {
        acc[t] = MFMA(w2[t], a[r][0], (f32x4{0.f, 0.f, 0.f, 0.f}));
        acc[t] = MFMA(w2[5 + t], a[r][1], acc[t]);
      }
      if (w < 4) {
#pragma unroll
        for (int t = 0; t < 5; ++t) acc[t] = MFMA(w2[10 + t], a[r][2], acc[t]);
      }
#pragma unroll
      for (int t = 0; t < 5; ++t) *(lds_f32x4*)(smem + ((r * 5 + t) * 8 + w) * 1024 + lane16) = acc[t];
    }
    BAR();
#pragma unroll
    for (int n = 0; n < 3; ++n) {
      if (n < 2 || w < 4) {
        const int u = w + 8 * n, rt = u / 5, t = u - 5 * rt;
        f32x4 s = {bf_lo(res[n][0]), bf_hi(res[n][0]), bf_lo(res[n][1]), bf_hi(res[n][1])};
#pragma unroll
        for (int i = 0; i < 8; ++i) s += *(lds_f32x4*)(smem + (u * 8 + i) * 1024 + lane16);
        res[n] = u32x2{pk_bf16(s[0], s[1]), pk_bf16(s[2], s[3])};
        st8(rs, res[n], (lq >> 1) * 256 + li * 16 + (lq & 1) * 8, WS_H + (rt0 + rt) * 20 * 1024 + (10 * p + 2 * t) * 256);
      }
    }
  }
  publish(ws, 1, g, j, tag);
  STAMP(6);
  if (w == 7) wait_group(ws, 1, g, lane, tag);
  BAR();
  STAMP(7);
  u32x4 w4[35];
  const int ks4 = w < 6 ? 7 * w : 42 + 6 * (w - 6);
  const unsigned w4off = WS_W4 + (p * 270 + ks4 * 5) * 1024;
  {
    const unsigned hb = WS_H + (8 * g + w) * 20 * 1024;
    u32x4 hs[20];
    f32x4 ag[8], au[8], cg = {0.f, 0.f, 0.f, 0.f}, cu = {0.f, 0.f, 0.f, 0.f};
    const u32x4 ones = {0x3f803f80u, 0x3f803f80u, 0x3f803f80u, 0x3f803f80u};
    float s1 = 0.f, s2 = 0.f;
#pragma unroll
    for (int k = 0; k < 8; ++k) hs[k] = ld16(rs, lane16, hb + k * 1024);
#pragma unroll
    for (int c = 0; c < 5; ++c) {
#pragma unroll
      for (int k = 4 * c; k < 4 * c + 4; ++k) {
        *(lds_u32x4*)(smem + (w * 20 + k) * 1024 + lane16) = hs[k];
#pragma unroll
        for (int e = 0; e < 4; ++e) { s1 = dot2(hs[k][e], 0x3f803f80u, s1); s2 = dot2(hs[k][e], hs[k][e], s2); }
      }
      asm volatile("" : "+v"(s1), "+v"(s2));
      if (c < 3) {
#pragma unroll
        for (int k = 4 * c + 8; k < 4 * c + 12; ++k) hs[k] = ld16(rs, lane16, hb + k * 1024);
      }
      BAR();
      {
#pragma unroll
        for (int k = 4 * c; k < 4 * c + 4; ++k) {
#pragma unroll
          for (int r = 0; r < 8; ++r) {
            u32x4 a = *(lds_u32x4*)(smem + (r * 20 + k) * 1024 + lane16);
            ag[r] = MFMA(w3[0][k], a, k == 0 ? (f32x4{0.f, 0.f, 0.f, 0.f}) : ag[r]);
            au[r] = MFMA(w3[1][k], a, k == 0 ? (f32x4{0.f, 0.f, 0.f, 0.f}) : au[r]);
          }
          cg = MFMA(w3[0][k], ones, cg);
          cu = MFMA(w3[1][k], ones, cu);
        }
      }
      CFENCE();
      if (c < 2) {
#pragma unroll
        for (int k = 12 + 4 * c; k < 16 + 4 * c; ++k) { w3[0][k] = ld16(rs, lane16, w3off + k * 1024); w3[1][k] = ld16(rs, lane16, w3off + (20 + k) * 1024); }
      } else {
#pragma unroll
        for (int k = (c - 2) * 12; k < (c == 4 ? 35 : (c - 1) * 12); ++k) w4[k] = ld16(rs, lane16, w4off + ((k < 30 || w < 6) ? k : 0) * 1024);
      }
      CFENCE();
    }
    s1 += __shfl_xor(s1, 16); s2 += __shfl_xor(s2, 16);
    s1 += __shfl_xor(s1, 32); s2 += __shfl_xor(s2, 32);
    const float mean = s1 * (1.f / D), var = fmaxf(s2 * (1.f / D) - mean * mean, 0.f);
    if (lq == 0) *(lds_f32x2*)(smem + (w * 16 + li) * 8) = f32x2{mean, rsqrtf(var + 1e-5f)};
    BAR();
    if (act3) {
#pragma unroll
      for (int r = 0; r < 8; ++r) {
        const f32x2 st = *(lds_f32x2*)(smem + (r * 16 + li) * 8);
        float y[4];
#pragma unroll
        for (int e = 0; e < 4; ++e) {
          const float gg = (ag[r][e] - st[0] * cg[e]) * st[1], uu = (au[r][e] - st[0] * cu[e]) * st[1];
          y[e] = gg * __builtin_amdgcn_rcpf(1.f + __expf(-gg)) * uu;
        }
        st8(rs, u32x2{pk_bf16(y[0], y[1]), pk_bf16(y[2], y[3])}, (lq >> 1) * 256 + li * 16 + (lq & 1) * 8,
            WS_A + (8 * g + r) * 54 * 1024 + ft * 512);
      }
    }
  }
  publish(ws, 2, g, j, tag);
  STAMP(8);
  if (w == 7) wait_group(ws, 2, g, lane, tag);
  BAR();
  STAMP(9);
  if (b == 0 && tid == 0) __hip_atomic_store((gu64*)(ws + WS_EPOCH), tag, RLX_AGENT);
  {
    u32x4 a[4][7];
#pragma unroll
    for (int r = 0; r < 2; ++r)
#pragma unroll
      for (int k = 0; k < 7; ++k) a[r][k] = ld16(rs, lane16, WS_A + ((rt0 + r) * 54 + ks4 + ((k < 6 || w < 6) ? k : 0)) * 1024);
#pragma unroll
    for (int r = 0; r < 4; ++r) {
      f32x4 acc[5];
#pragma unroll
      for (int k = 0; k < 6; ++k)
#pragma unroll
        for (int t = 0; t < 5; ++t) acc[t] = MFMA(w4[k * 5 + t], a[r][k], k == 0 ? (f32x4{0.f, 0.f, 0.f, 0.f}) : acc[t]);
      if (w < 6) {
#pragma unroll
        for (int t = 0; t < 5; ++t) acc[t] = MFMA(w4[30 + t], a[r][6], acc[t]);
      }
      if (r < 2) {
        CFENCE();
#pragma unroll
        for (int k = 0; k < 7; ++k) a[r + 2][k] = ld16(rs, lane16, WS_A + ((rt0 + r + 2) * 54 + ks4 + ((k < 6 || w < 6) ? k : 0)) * 1024);
      }
#pragma unroll
      for (int t = 0; t < 5; ++t) *(lds_f32x4*)(smem + ((r * 5 + t) * 8 + w) * 1024 + lane16) = acc[t];
    }
    BAR();
#pragma unroll
    for (int n = 0; n < 3; ++n) {
      if (n < 2 || w < 4) {
        const int u = w + 8 * n, rt = u / 5, t = u - 5 * rt;
        f32x4 s = {bf_lo(res[n][0]), bf_hi(res[n][0]), bf_lo(res[n][1]), bf_hi(res[n][1])};
#pragma unroll
        for (int i = 0; i < 8; ++i) s += *(lds_f32x4*)(smem + (u * 8 + i) * 1024 + lane16);
        int l2 = lane16;
        asm volatile("" : "+v"(l2));
        *(u32x2*)(out + ((rt0 + rt) * 16 + ((l2 >> 4) & 15)) * D + 80 * p + 16 * t + 4 * (l2 >> 8)) = u32x2{pk_bf16(s[0], s[1]), pk_bf16(s[2], s[3])};
      }
    }
  }
  STAMP(10); STAMPC(15);
}

extern "C" void kernel_launch(void* const* d_in, const int* in_sizes, int n_in, void* d_out, int out_size,
                              void* d_ws, size_t ws_size, hipStream_t stream) {
  fused_block<<<256, 512, 0, stream>>>((const unsigned short*)d_in[0], (const unsigned short*)d_in[1],
                                       (const unsigned short*)d_in[2], (const unsigned short*)d_in[4],
                                       (const unsigned short*)d_in[5], (const unsigned short*)d_in[6],
                                       (unsigned short*)d_out, (char*)d_ws);
}
```

```cpp
#include <hip/hip_runtime.h>
#include <hip/hip_bf16.h>
#include <stdint.h>
#include <type_traits>

typedef __attribute__((ext_vector_type(8))) __bf16 bf16x8;
typedef __attribute__((ext_vector_type(2))) __bf16 bf16x2;
typedef __attribute__((ext_vector_type(4))) float f32x4;
typedef __attribute__((ext_vector_type(2))) float f32x2;
typedef __attribute__((ext_vector_type(4))) unsigned u32x4;
typedef __attribute__((ext_vector_type(2))) unsigned u32x2;
typedef unsigned long long u64;
typedef __attribute__((address_space(1))) u64 gu64;
typedef __attribute__((address_space(3))) u32x4 lds_u32x4;
typedef __attribute__((address_space(3))) u32x2 lds_u32x2;
typedef __attribute__((address_space(3))) f32x4 lds_f32x4;
typedef __attribute__((address_space(3))) f32x2 lds_f32x2;
typedef __attribute__((address_space(3))) char lds_char;

constexpr int S = 2048, D = 640, F = 1728;
constexpr unsigned WS_EPOCH = 0;
constexpr unsigned WS_FLAGW = 4096;
constexpr unsigned WS_FLAG = 32768;
constexpr unsigned WS_PROF = 131072;
constexpr unsigned WS_W1 = 1u << 18;
constexpr unsigned WS_W2 = WS_W1 + 16 * 160 * 1024;
constexpr unsigned WS_W4 = WS_W2 + 8 * 100 * 1024;
constexpr unsigned WS_W3 = WS_W4 + 8 * 270 * 1024;
constexpr unsigned WS_O = WS_W3 + 108 * 40 * 1024;
constexpr unsigned WS_H = WS_O + 128 * 20 * 1024;
constexpr unsigned WS_A = WS_H + 128 * 20 * 1024;
constexpr unsigned WS_S = WS_A + 128 * 54 * 1024;
constexpr unsigned WS_END = WS_S + 16 * 40 * 1024;
constexpr unsigned SPIN_CAP = 1u << 22;
constexpr int QKV = 40960, W3PRE = 12, CODE_KB = 24;

#define RLX_AGENT __ATOMIC_RELAXED, __HIP_MEMORY_SCOPE_AGENT
#define BAR() asm volatile("s_waitcnt lgkmcnt(0)\n\ts_barrier" ::: "memory")
#define DRAIN() asm volatile("s_waitcnt vmcnt(0)" ::: "memory")
#define CFENCE() do { asm volatile("" ::: "memory"); __builtin_amdgcn_sched_barrier(0); } while (0)
#define MFMA(a, b, c) __builtin_amdgcn_mfma_f32_16x16x32_bf16(__builtin_bit_cast(bf16x8, a), __builtin_bit_cast(bf16x8, b), c, 0, 0, 0)
#define STAMPC(i)
#define STAMP(i)

__device__ __forceinline__ unsigned pk_bf16(float a, float b) {
  f32x2 v = {a, b};
  return __builtin_bit_cast(unsigned, __builtin_convertvector(v, bf16x2));
}
__device__ __forceinline__ float bf_lo(unsigned u) { return __builtin_bit_cast(float, u << 16); }
__device__ __forceinline__ float bf_hi(unsigned u) { return __builtin_bit_cast(float, u & 0xffff0000u); }
__device__ __forceinline__ float dot2(unsigned a, unsigned b, float c) {
  return __builtin_amdgcn_fdot2_f32_bf16(__builtin_bit_cast(bf16x2, a), __builtin_bit_cast(bf16x2, b), c, false);
}
template <int CTRL> __device__ __forceinline__ float dpp(float v) {
  return __builtin_bit_cast(float, __builtin_amdgcn_update_dpp(0, __builtin_bit_cast(int, v), CTRL, 0xf, 0xf, true));
}
__device__ __forceinline__ float sum16(float v) {
  v += dpp<0xB1>(v);
  v += dpp<0x4E>(v);
  v += dpp<0x141>(v);
  v += dpp<0x140>(v);
  return v;
}
__device__ __forceinline__ float sum_rows(float v) {
  unsigned u = __builtin_bit_cast(unsigned, v);
  auto a = __builtin_amdgcn_permlane16_swap(u, u, false, false);
  u = __builtin_bit_cast(unsigned, __builtin_bit_cast(float, (unsigned)a[0]) + __builtin_bit_cast(float, (unsigned)a[1]));
  auto c = __builtin_amdgcn_permlane32_swap(u, u, false, false);
  return __builtin_bit_cast(float, (unsigned)c[0]) + __builtin_bit_cast(float, (unsigned)c[1]);
}
typedef __amdgpu_buffer_rsrc_t rsrc_t;
__device__ __forceinline__ u32x4 ld16(rsrc_t r, unsigned voff, unsigned soff) {
  return __builtin_amdgcn_raw_buffer_load_b128(r, voff, soff, 16);
}
__device__ __forceinline__ void dma16(rsrc_t r, unsigned voff, unsigned soff, unsigned ldsaddr) {
  asm volatile("s_mov_b32 m0, %3\n\ts_nop 0\n\tbuffer_load_dwordx4 %0, %1, %2 offen sc1 lds"
               ::"v"(voff), "s"(r), "s"(soff), "s"(ldsaddr) : "memory");
}
__device__ __forceinline__ void st16(rsrc_t r, u32x4 v, unsigned voff, unsigned soff) {
  __builtin_amdgcn_raw_buffer_store_b128(v, r, voff + soff, 0, 16);
}
__device__ __forceinline__ void st8(rsrc_t r, u32x2 v, unsigned voff, unsigned soff) {
  __builtin_amdgcn_raw_buffer_store_b64(v, r, voff, soff, 16);
}
__device__ __forceinline__ void wait_group(char* ws, int phase, int g, int lane, u64 tag) {
  gu64* f = (gu64*)(ws + WS_FLAG + (unsigned)(phase * 256 + g * 16 + (lane & 15)) * 64u);
#pragma unroll 1
  for (unsigned spins = 0; spins < SPIN_CAP; ++spins) {
    u64 v = __hip_atomic_load(f, RLX_AGENT);
    if (__all(v == tag)) break;
    __builtin_amdgcn_s_sleep(1);
  }
  CFENCE();
}
#define VMWAIT(n) asm volatile("s_waitcnt vmcnt(" #n ")" ::: "memory")
__device__ __forceinline__ void st8_one(rsrc_t r, bool me, u64 v, unsigned off) {
  st8(r, u32x2{(unsigned)v, (unsigned)(v >> 32)}, me ? 0u : 0x80000000u, off);
}
#define raise(phase) st8_one(rs, tid == 448, tag, WS_FLAG + (unsigned)((phase) * 256 + g * 16 + j) * 64u)

__global__ void __launch_bounds__(512, 2)
fused_block(const unsigned short* __restrict__ x, const unsigned short* __restrict__ wqkv,
            const unsigned short* __restrict__ wo, const unsigned short* __restrict__ wg,
            const unsigned short* __restrict__ wu, const unsigned short* __restrict__ wd,
            unsigned short* __restrict__ out, char* ws) {
  __shared__ __attribute__((aligned(16))) char smem_[163840];
  lds_char* smem = (lds_char*)smem_;
  const int tid = threadIdx.x, lane = tid & 63;
  const int w = __builtin_amdgcn_readfirstlane(tid >> 6);
  const int b = blockIdx.x, xcd = b & 7, slot = b >> 3;
  const int g = 4 * (xcd >> 1) + (slot >> 3), j = 8 * (xcd & 1) + (slot & 7), p = j >> 1;
  const int li = lane & 15, lq = lane >> 4;
  const rsrc_t rs = __builtin_amdgcn_make_buffer_rsrc(ws, 0, WS_END, 0x00020000);
  const unsigned lane16 = lane * 16;
  STAMP(0); STAMPC(30);
  const u64 tagv = __hip_atomic_load((gu64*)(ws + WS_EPOCH), RLX_AGENT) + 1;

  const int ftbase = 27 * (p >> 1) + 14 * (p & 1), nft = 14 - (p & 1);
  u32x4 rv[5];
  unsigned rdst[5];
  {
    const int u = (g * 2 + (j & 1)) * 8 + w, kr = lane >> 1, ch = (lane & 1) * 8;
#pragma unroll
    for (int r = 0; r < 5; ++r) {
      int ty, f;
      if (r == 0) { ty = 0; f = u; }
      else if (r == 1) { ty = 3; f = u; }
      else if (r == 2) { ty = 3; f = u + 256; }
      else if (r == 3) { ty = 2; f = u; }
      else {
        ty = u < 64 ? 0 : u < 164 ? 1 : u < 178 ? 2 : 3;
        f = u < 64 ? 256 + u : u < 164 ? u - 64 : u < 178 ? 256 + u - 164 : 512 + u - 178;
        if (ty == 3 && f >= 40 * nft) f = u;
      }
      const unsigned short* src;
      if (ty == 0) {
        const int ks = f >> 4, jj = 2 * p + ((f >> 3) & 1), t = f & 7;
        int c = 16 * t + ch; c = c >= 120 ? 112 : c;
        const int part = c / 40;
        src = wqkv + (32 * ks + kr) * 1920 + part * 600 + 40 * jj + c; rdst[r] = WS_W1 + ((jj * 8 + t) * 20 + ks) * 1024;
      } else if (ty == 1) {
        const int ks = f / 5, t = f - ks * 5, O = 4 * ks + lq, jj = O / 5, o = O - jj * 5, e = kr & 7;
        const int row = 40 * jj + e + (o < 4 ? 20 * (o >> 1) + 8 * (o & 1) : e < 4 ? 16 : 32);
        src = wo + row * 640 + 80 * p + 16 * t + ch; rdst[r] = WS_W2 + (p * 100 + f) * 1024;
      } else if (ty == 2) {
        const int ks = f / 5, t = f - ks * 5;
        src = wd + (32 * ks + kr) * 640 + 80 * p + 16 * t + ch; rdst[r] = WS_W4 + (p * 270 + f) * 1024;
      } else {
        const int ks = (p & 1) ? f / 26 : f / 28, rem = f - ks * 2 * nft, m = rem >= nft, ft = ftbase + rem - m * nft;
        src = (m ? wu : wg) + (32 * ks + kr) * F + 16 * ft + ch; rdst[r] = WS_W3 + ((ft * 2 + m) * 20 + ks) * 1024;
      }
      rv[r] = *(const u32x4*)src;
    }
  }
  CFENCE();
  unsigned code_pf = 0;
  if (w == 7) {
    const unsigned* pc = (const unsigned*)(__builtin_amdgcn_s_getpc() & ~127ull) + lane * 32;
    unsigned t = 0;
#pragma unroll
    for (int i = 0; i < CODE_KB / 8; ++i) t |= __builtin_nontemporal_load(pc + i * 2048) ;
    code_pf = t;
  }
  CFENCE();
#pragma unroll
  for (int r = 0; r < 5; ++r) {
    lds_char* sc = smem + 155648 + w * 1024;
    *(lds_u32x4*)(sc + lane16) = rv[r];
    typedef __attribute__((ext_vector_type(4))) short s16x4;
    typedef __attribute__((address_space(3))) s16x4 lds_s16x4;
    const u32x2 lo = __builtin_bit_cast(u32x2, __builtin_amdgcn_ds_read_tr16_b64_v4i16((lds_s16x4*)(sc + lq * 256 + li * 8)));
    const u32x2 hi = __builtin_bit_cast(u32x2, __builtin_amdgcn_ds_read_tr16_b64_v4i16((lds_s16x4*)(sc + lq * 256 + li * 8 + 128)));
    asm volatile("" ::: "memory");
    st16(rs, u32x4{lo[0], lo[1], hi[0], hi[1]}, lane16, rdst[r]);
  }
  CFENCE();
  u32x4 xr[5][5];
#pragma unroll
  for (int i = 0; i < 5; ++i) {
    int row = 128 * g - 16 + 4 * (w + 8 * i) + lq;
    row = row < 0 ? 0 : row;
    const u32x4* xp = (const u32x4*)(x + row * D) + li;
    if (i < 4 || w < 4) {
#pragma unroll
      for (int c = 0; c < 5; ++c) xr[i][c] = xp[16 * c];
    }
  }
  CFENCE();
  const u64 tag = ((u64)__builtin_amdgcn_readfirstlane((unsigned)(tagv >> 32)) << 32) | (unsigned)__builtin_amdgcn_readfirstlane((unsigned)tagv);
  STAMP(11);
  asm volatile("" ::"v"(code_pf));
  VMWAIT(20);
  if (lane == 0) __hip_atomic_store((gu64*)(ws + WS_FLAGW + (unsigned)((p * 32 + g * 2 + (j & 1)) * 8 + w) * 8u), tag, RLX_AGENT);
  STAMP(1);
  const unsigned xw = ((li >> 2) * 9 + (w >> 2)) * 1024 + ((li & 3) * 16 + ((4 * (w & 3) + lq) ^ (li & 7))) * 16;
#pragma unroll
  for (int i = 0; i < 5; ++i) {
    if (i < 4 || w < 4) {
      float s1 = 0.f, s2 = 0.f;
#pragma unroll
      for (int c = 0; c < 5; ++c) {
        if (c < 4) *(lds_u32x4*)(smem + xw + (36 * c + 2 * i) * 1024) = xr[i][c];
#pragma unroll
        for (int e = 0; e < 4; ++e) { s1 = dot2(xr[i][c][e], 0x3f803f80u, s1); s2 = dot2(xr[i][c][e], xr[i][c][e], s2); }
      }
      const float mean = sum16(s1) * (1.f / D), var = fmaxf(sum16(s2) * (1.f / D) - mean * mean, 0.f);
      if (li == 0) *(lds_f32x2*)(smem + 147456 + (4 * (w + 8 * i) + lq) * 8) = f32x2{mean, rsqrtf(var + 1e-5f)};
    }
  }
  STAMP(12);
  if (w == 7) {
  #pragma unroll 1
  for (unsigned spins = 0; spins < SPIN_CAP; ++spins) {
      u32x4 a = ld16(rs, lane16, WS_FLAGW + p * 2048), c = ld16(rs, lane16, WS_FLAGW + p * 2048 + 1024);
      const unsigned tl = (unsigned)tag, th = (unsigned)(tag >> 32);
      bool ok = a[0] == tl && a[1] == th && a[2] == tl && a[3] == th && c[0] == tl && c[1] == th && c[2] == tl && c[3] == th;
      if (__all(ok)) break;
      __builtin_amdgcn_s_sleep(1);
      CFENCE();
    }
    }
  BAR();
  STAMP(2);
  u32x4 w1[20], w2[15], w3[2][20];
  const u32x4 ones = {0x3f803f80u, 0x3f803f80u, 0x3f803f80u, 0x3f803f80u};
  const f32x4 zero4 = {0.f, 0.f, 0.f, 0.f};
  const int ks2 = w < 4 ? 3 * w : 12 + 2 * (w - 4);
  const int ft = ftbase + 7 * (j & 1) + w;
  const bool act3 = w < 7 && 7 * (j & 1) + w < nft;
  const unsigned w3off = WS_W3 + (act3 ? ft : ftbase) * 40 * 1024;
  const unsigned l16a = act3 ? lane16 : 0x80000000u, l16b = w < 4 ? lane16 : 0x80000000u;
#define LDW3(n) w3[(n) & 1][(n) >> 1] = ld16(rs, l16a, w3off + (((n) & 1) * 20 + ((n) >> 1)) * 1024)
  const unsigned w1off = WS_W1 + (j * 8 + w) * 20 * 1024;
#pragma unroll
  for (int k = 0; k < 4; ++k) w1[k] = ld16(rs, lane16 + k * 1024, w1off);
  CFENCE();
  {
    f32x4 acc[9], cs;
    u32x4 af[2][9];
    const unsigned ra0 = (lq * 16 + (li ^ lq)) * 16, ra1 = (lq * 16 + (li ^ (lq + 4))) * 16;
#pragma unroll
    for (int r = 0; r < 9; ++r) af[0][r] = *(lds_u32x4*)(smem + ra0 + r * 1024);
#pragma unroll
    for (int k = 0; k < 20; ++k) {
      if (k == 15) { BAR(); STAMP(15); }
      if (k < 16) w1[k + 4] = ld16(rs, lane16 + (k & 3) * 1024, w1off + ((k + 4) & ~3) * 1024);
      if (k >= 1 && k < 16) w2[k - 1] = ld16(rs, k < 11 ? lane16 : l16b, WS_W2 + (p * 100 + ks2 * 5 + k - 1) * 1024);
      if (k >= 16) { LDW3(2 * k - 32); LDW3(2 * k - 31); }
      CFENCE();
      if (k < 19) {
#pragma unroll
        for (int r = 0; r < 9; ++r) af[(k + 1) & 1][r] = *(lds_u32x4*)(smem + ((k & 1) ? ra0 : ra1) + (((k + 1) & 15) * 9 + r) * 1024);
      }
#pragma unroll
      for (int r = 0; r < 9; ++r) acc[r] = MFMA(w1[k], af[k & 1][r], k == 0 ? zero4 : acc[r]);
      cs = MFMA(w1[k], ones, k == 0 ? zero4 : cs);
#pragma unroll
      for (int r = 0; r < 9; ++r) { __builtin_amdgcn_sched_group_barrier(0x100, 1, 0); __builtin_amdgcn_sched_group_barrier(0x8, 1, 0); }
      CFENCE();
      if (k == 3) {
        BAR(); STAMP(14);
#pragma unroll
        for (int i = 0; i < 5; ++i)
          if (i < 4 || w < 4) *(lds_u32x4*)(smem + xw + 2 * i * 1024) = xr[i][4];
      }
    }
    STAMP(16);
    const int c4 = 4 * w + lq, seg = c4 / 5, sub = c4 - 5 * seg;
    lds_char* qb = smem + QKV + li * 368 + (w < 5 ? 80 + (seg & 1) * 200 + (seg >> 1) * 40 + sub * 8 : seg < 6 ? (seg & 1) * 192 + sub * 16 : 160 + sub * 16);
#pragma unroll
    for (int r = 0; r < 9; ++r) {
      if (r < 4) LDW3(8 + r);
      const f32x2 st = *(lds_f32x2*)(smem + 147456 + (16 * r + li) * 8);
      const f32x4 y = (acc[r] - st[0] * cs) * st[1];
      if (w < 5) *(lds_u32x2*)(qb + r * 16 * 368) = u32x2{pk_bf16(y[0], y[1]), pk_bf16(y[2], y[3])};
      else *(lds_f32x4*)(qb + r * 16 * 368) = y;
    }
  }
  BAR();
  STAMP(3);
  const int rtb = 8 * g + 2 * (j & 1);
  const int un[3] = {w, 10 + w, w < 2 ? 8 + w : 16 + (w & 3)};
  u32x2 res[3];
  {
    const int tok = tid >> 2, hd = (tid >> 1) & 1, hf = tid & 1;
    lds_char* base = smem + QKV + (16 + tok) * 368;
    u32x2 qv[5];
#pragma unroll
    for (int c = 0; c < 5; ++c) qv[c] = *(lds_u32x2*)(base + 80 + hd * 200 + c * 8);
    lds_char* kb = base - (hf * 8 + 6) * 368 + hd * 200;
    lds_char* vb = kb - hd * 8;
    float lg[4];
#pragma unroll
    for (int i = 0; i < 4; ++i) {
      float s = 0.f;
#pragma unroll
      for (int c = 0; c < 5; ++c) {
        u32x2 kv = *(lds_u32x2*)(kb + (6 - 2 * i) * 368 + 120 + c * 8);
        s = dot2(qv[c][0], kv[0], s); s = dot2(qv[c][1], kv[1], s);
      }
      lg[i] = (128 * g + tok - 8 * hf - 2 * i >= 0) ? s * 0.22360679774997896f : -1e9f;
    }
    { LDW3(12); LDW3(13); LDW3(14); LDW3(15); }
#pragma unroll
    for (int n = 0; n < 3; ++n) {
      const int r = un[n] / 5, t = un[n] - 5 * r;
      res[n] = *(const u32x2*)(x + ((rtb + (r >> 1) * 4 + (r & 1)) * 16 + li) * D + 80 * p + 16 * t + 4 * lq);
    }
    float mx = fmaxf(fmaxf(lg[0], lg[1]), fmaxf(lg[2], lg[3]));
    mx = fmaxf(mx, dpp<0xB1>(mx));
    float sum = 0.f;
#pragma unroll
    for (int i = 0; i < 4; ++i) { lg[i] = __expf(lg[i] - mx); sum += lg[i]; }
    sum += dpp<0xB1>(sum);
    const float inv = __builtin_amdgcn_rcpf(sum);
    f32x4 o[5];
#pragma unroll
    for (int i = 0; i < 4; ++i) {
      const float pr = lg[i] * inv;
#pragma unroll
      for (int c = 0; c < 5; ++c) {
        const f32x4 vv = *(lds_f32x4*)(vb + (6 - 2 * i) * 368 + c * 16);
        o[c] = i == 0 ? pr * vv : o[c] + pr * vv;
      }
    }
    float e8[8], t4[4];
#pragma unroll
    for (int e = 0; e < 8; ++e) {
      const float lo = o[e >> 2][e & 3], hi = o[2 + (e >> 2)][e & 3];
      e8[e] = (hf ? hi : lo) + dpp<0xB1>(hf ? lo : hi);
    }
#pragma unroll
    for (int e = 0; e < 4; ++e) t4[e] = o[4][e] + dpp<0xB1>(o[4][e]);
    const unsigned ob = WS_O + (8 * g + w) * 20 * 1024;
    st16(rs, u32x4{pk_bf16(e8[0], e8[1]), pk_bf16(e8[2], e8[3]), pk_bf16(e8[4], e8[5]), pk_bf16(e8[6], e8[7])},
         (5 * j + 2 * hd + hf) * 256 + (tok & 15) * 16, ob);
    if (!hf) st8(rs, u32x2{pk_bf16(t4[0], t4[1]), pk_bf16(t4[2], t4[3])}, (5 * j + 4) * 256 + (tok & 15) * 16 + hd * 8, ob);
  }
  STAMP(17);
  DRAIN();
  BAR();
  raise(0);
  STAMP(4);
  {
#pragma unroll
    for (int n = 16; n < 2 * W3PRE; ++n) LDW3(n);
  }
  if (w == 7) wait_group(ws, 0, g, lane, tag);
  BAR();
  STAMP(5);
  const unsigned cst = (lq >> 1) * 256 + li * 16 + (lq & 1) * 8;
  {
    u32x4 a[4][3];
#pragma unroll
    for (int r = 0; r < 4; ++r)
#pragma unroll
      for (int k = 0; k < 3; ++k)
        a[r][k] = ld16(rs, (k < 2 ? lane16 : l16b) + k * 1024, WS_O + ((rtb + (r >> 1) * 4 + (r & 1)) * 20 + ks2) * 1024);
    CFENCE();
#pragma unroll
    for (int r = 0; r < 4; ++r) {
      f32x4 acc[5];
#pragma unroll
      for (int t = 0; t < 5; ++t) acc[t] = MFMA(w2[t], a[r][0], zero4);
#pragma unroll
      for (int t = 0; t < 5; ++t) acc[t] = MFMA(w2[5 + t], a[r][1], acc[t]);
      if (w < 4) {
#pragma unroll
        for (int t = 0; t < 5; ++t) acc[t] = MFMA(w2[10 + t], a[r][2], acc[t]);
      }
#pragma unroll
      for (int t = 0; t < 5; ++t) *(lds_f32x4*)(smem + ((r * 5 + t) * 8 + w) * 1024 + lane16) = acc[t];
    }
    STAMP(18);
    BAR();
#pragma unroll
    for (int n = 0; n < 3; ++n) {
      if (n < 2 || w < 4) {
        const int r = un[n] / 5, t = un[n] - 5 * r;
        f32x4 s = {bf_lo(res[n][0]), bf_hi(res[n][0]), bf_lo(res[n][1]), bf_hi(res[n][1])};
#pragma unroll
        for (int i = 0; i < 8; ++i) s += *(lds_f32x4*)(smem + (un[n] * 8 + i) * 1024 + lane16);
        res[n] = u32x2{pk_bf16(s[0], s[1]), pk_bf16(s[2], s[3])};
        st8(rs, res[n], cst, WS_H + (rtb + (r >> 1) * 4 + (r & 1)) * 20 * 1024 + (10 * p + 2 * t) * 256);
        const float s1 = sum_rows(dot2(res[n][0], 0x3f803f80u, dot2(res[n][1], 0x3f803f80u, 0.f)));
        const float s2 = sum_rows(dot2(res[n][0], res[n][0], dot2(res[n][1], res[n][1], 0.f)));
        if (lq == 0) st8(rs, u32x2{__builtin_bit_cast(unsigned, s1), __builtin_bit_cast(unsigned, s2)}, li * 8,
                         WS_S + (g * 40 + 5 * p + t) * 1024 + (2 * (j & 1) + (r >> 1) * 4 + (r & 1)) * 128);
      }
    }
  }
  STAMP(19);
  DRAIN();
  BAR();
  raise(1);
  STAMP(6);
  u32x4 w4[35];
  const int ks4 = w < 6 ? 7 * w : 42 + 6 * (w - 6);
  const unsigned w4off = WS_W4 + (p * 270 + ks4 * 5) * 1024;
  const unsigned lds0 = (unsigned)(size_t)smem, hb = WS_H + 8 * g * 20 * 1024;
#define L16C (w < 6 ? lane16 : 0x80000000u)
#define LDW4(n) do { if ((n) < 35) w4[n] = ld16(rs, (n) < 30 ? lane16 : L16C, w4off + (n) * 1024); } while (0)
#define DMA_A() do { _Pragma("unroll") for (int i = 0; i < 10; ++i) \
    dma16(rs, lane16, hb + ((w & 3) * 20 + 2 * i + (w >> 2)) * 1024, lds0 + (8 * i + w) * 1024); VMWAIT(7); BAR(); } while (0)
  if (w == 7) {
    wait_group(ws, 1, g, lane, tag);
    BAR();
    DMA_A();
    f32x4 sv = zero4;
    {
      u32x4 sp[20];
#pragma unroll
      for (int n = 0; n < 20; ++n) sp[n] = ld16(rs, lane16 + (n & 3) * 1024, WS_S + (g * 40 + (n & ~3)) * 1024);
#pragma unroll
      for (int n = 0; n < 20; ++n) sv += __builtin_bit_cast(f32x4, sp[n]);
      CFENCE();
#pragma unroll
      for (int n = 0; n < 20; ++n) sp[n] = ld16(rs, lane16 + (n & 3) * 1024, WS_S + (g * 40 + 20 + (n & ~3)) * 1024);
      VMWAIT(20); BAR();
#pragma unroll 1
      for (int f = 0; f < 79; ++f) dma16(rs, lane16, hb + ((4 + (f & 3)) * 20 + (f >> 2)) * 1024, lds0 + (80 + f) * 1024);
#pragma unroll
      for (int n = 0; n < 20; ++n) sv += __builtin_bit_cast(f32x4, sp[n]);
    }
    const float m0 = sv[0] * (1.f / D), m1 = sv[2] * (1.f / D);
    *(lds_f32x4*)(smem + 162816 + lane16) = f32x4{m0, rsqrtf(fmaxf(sv[1] * (1.f / D) - m0 * m0, 0.f) + 1e-5f),
                                                  m1, rsqrtf(fmaxf(sv[3] * (1.f / D) - m1 * m1, 0.f) + 1e-5f)};
    VMWAIT(0); BAR();
    dma16(rs, lane16, hb + 159 * 1024, lds0);
#pragma unroll
    for (int n = 0; n < 30; ++n) w3[n & 1][n >> 1] = ld16(rs, lane16, w4off + n * 1024);
    VMWAIT(30);
    BAR();
    raise(2);
    wait_group(ws, 2, g, lane, tag);
    BAR();
  }
#pragma unroll
  for (int n = 0; n < 35; ++n) w4[n] = w3[n & 1][n < 30 ? n >> 1 : 0];
  int wo_ = w;
  asm volatile("" : "+s"(wo_));
  if (wo_ < 7) {
    {
#pragma unroll
      for (int n = 2 * W3PRE; n < 40; ++n) LDW3(n);
    }
    f32x4 cg = zero4, cu = zero4;
#pragma unroll
    for (int k = 0; k < W3PRE; ++k) { cg = MFMA(w3[0][k], ones, cg); cu = MFMA(w3[1][k], ones, cu); }
    CFENCE();
    BAR();
    STAMP(7);
    DMA_A();
    STAMP(13);
    if (w < 4) __builtin_amdgcn_s_setprio(1);
    f32x4 ag[8], au[8];
    u32x4 af[2][4];
    auto epi = [&](int r) {
      const f32x2 st = *(lds_f32x2*)(smem + 162816 + (r * 16 + li) * 8);
      const f32x4 gg = (ag[r] - st[0] * cg) * st[1], uu = (au[r] - st[0] * cu) * st[1];
      float y[4];
#pragma unroll
      for (int e = 0; e < 4; ++e) y[e] = gg[e] * __builtin_amdgcn_rcpf(1.f + __expf(-gg[e])) * uu[e];
      if (act3) st8(rs, u32x2{pk_bf16(y[0], y[1]), pk_bf16(y[2], y[3])}, cst, WS_A + (8 * g + r) * 54 * 1024 + ft * 512);
    };
#pragma unroll
    for (int r = 0; r < 4; ++r) af[0][r] = *(lds_u32x4*)(smem + r * 1024 + lane16);
    lds_char* lb = smem + lane16;
    auto halfbody = [&](auto half_) {
     constexpr int h4 = decltype(half_)::value * 4;
#pragma unroll
     for (int k = 0; k < 20; ++k) {
      const int kk = h4 * 5 + k;
      if (kk == 5) { VMWAIT(0); BAR(); }
      if (kk == 14) { BAR(); STAMP(20); }
      if (kk == 28) { VMWAIT(14); BAR(); }
      if (kk > 20) { LDW4(2 * k - 2); LDW4(2 * k - 1); }
      CFENCE();
      if (kk == 15 || kk == 31) { lb += 65536; asm volatile("" : "+v"(lb)); }
      if (kk < 39) {
#pragma unroll
        for (int r = 0; r < 4; ++r)
          af[(kk + 1) & 1][r] = kk == 38 && r == 3 ? *(lds_u32x4*)(smem + lane16) : *(lds_u32x4*)(lb + (((kk + 1) * 4 + r) & 63) * 1024);
      }
#pragma unroll
      for (int r = 0; r < 4; ++r) {
        ag[h4 + r] = MFMA(w3[0][k], af[kk & 1][r], k == 0 ? zero4 : ag[h4 + r]);
        au[h4 + r] = MFMA(w3[1][k], af[kk & 1][r], k == 0 ? zero4 : au[h4 + r]);
      }
      if (kk >= 5 && kk < 25 - W3PRE) { cg = MFMA(w3[0][(kk + W3PRE - 5) % 20], ones, cg); cu = MFMA(w3[1][(kk + W3PRE - 5) % 20], ones, cu); }
#pragma unroll
      for (int r = 0; r < 4; ++r) { __builtin_amdgcn_sched_group_barrier(0x100, 1, 0); __builtin_amdgcn_sched_group_barrier(0x8, 2, 0); }
      CFENCE();
     }
#pragma unroll
     for (int r = 0; r < 4; ++r) { epi(h4 + r); if (r & 1) CFENCE(); }
    };
    halfbody(std::integral_constant<int, 0>{});
    halfbody(std::integral_constant<int, 1>{});
    __builtin_amdgcn_s_setprio(0);
    STAMP(21);
    BAR();
  }
  {
    u32x4 a[2][7];
    auto lda = [&](int half) {
#pragma unroll
      for (int r = 0; r < 2; ++r)
#pragma unroll
        for (int k = 0; k < 7; ++k)
          a[r][k] = ld16(rs, (k < 6 ? lane16 : L16C) + (k & 3) * 1024, WS_A + ((rtb + half * 4 + r) * 54 + ks4 + (k & ~3)) * 1024);
      CFENCE();
    };
    auto gemm = [&](int half) {
#pragma unroll
      for (int r = 0; r < 2; ++r) {
        f32x4 acc[5];
#pragma unroll
        for (int k = 0; k < 6; ++k)
#pragma unroll
          for (int t = 0; t < 5; ++t) acc[t] = MFMA(w4[k * 5 + t], a[r][k], k == 0 ? zero4 : acc[t]);
        if (w < 6) {
#pragma unroll
          for (int t = 0; t < 5; ++t) acc[t] = MFMA(w4[30 + t], a[r][6], acc[t]);
        }
#pragma unroll
        for (int t = 0; t < 5; ++t) *(lds_f32x4*)(smem + (((half * 2 + r) * 5 + t) * 8 + w) * 1024 + lane16) = acc[t];
      }
      CFENCE();
    };
    auto fin = [&](int n) {
      const int r = un[n] / 5, t = un[n] - 5 * r;
      f32x4 s = {bf_lo(res[n][0]), bf_hi(res[n][0]), bf_lo(res[n][1]), bf_hi(res[n][1])};
#pragma unroll
      for (int i = 0; i < 8; ++i) s += *(lds_f32x4*)(smem + (un[n] * 8 + i) * 1024 + lane16);
      int l2 = lane16;
      asm volatile("" : "+v"(l2));
      *(u32x2*)(out + ((rtb + (r >> 1) * 4 + (r & 1)) * 16 + ((l2 >> 4) & 15)) * D + 80 * p + 16 * t + 4 * (l2 >> 8)) =
          u32x2{pk_bf16(s[0], s[1]), pk_bf16(s[2], s[3])};
    };
    CFENCE();
    lda(0);
#pragma unroll
    for (int n = 0; n < 3; ++n) {
      const int r = un[n] / 5, t = un[n] - 5 * r;
      res[n] = __builtin_amdgcn_raw_buffer_load_b64(rs, cst, WS_H + (rtb + (r >> 1) * 4 + (r & 1)) * 20 * 1024 + (10 * p + 2 * t) * 256, 16);
    }
    CFENCE();
    VMWAIT(17);
    BAR();
    raise(3);
    STAMP(8);
    gemm(0);
    if (w == 7) wait_group(ws, 3, g, lane, tag);
    BAR();
    STAMP(9);
    if (b == 0 && tid == 0) __hip_atomic_store((gu64*)(ws + WS_EPOCH), tag, RLX_AGENT);
    lda(1);
    fin(0);
    if (w < 2) fin(2);
    CFENCE();
    gemm(1);
    STAMP(22);
    BAR();
    fin(1);
    if (w == 2 || w == 3) fin(2);
  }
  STAMP(10); STAMPC(31);
}

extern "C" void kernel_launch(void* const* d_in, const int* in_sizes, int n_in, void* d_out, int out_size,
                              void* d_ws, size_t ws_size, hipStream_t stream) {
  fused_block<<<256, 512, 0, stream>>>((const unsigned short*)d_in[0], (const unsigned short*)d_in[1],
                                       (const unsigned short*)d_in[2], (const unsigned short*)d_in[4],
                                       (const unsigned short*)d_in[5], (const unsigned short*)d_in[6],
                                       (unsigned short*)d_out, (char*)d_ws);
}
```

```cpp
#include <hip/hip_runtime.h>
#include <hip/hip_bf16.h>
#include <stdint.h>
#include <type_traits>

typedef __attribute__((ext_vector_type(8))) __bf16 bf16x8;
typedef __attribute__((ext_vector_type(2))) __bf16 bf16x2;
typedef __attribute__((ext_vector_type(4))) float f32x4;
typedef __attribute__((ext_vector_type(2))) float f32x2;
typedef __attribute__((ext_vector_type(4))) unsigned u32x4;
typedef __attribute__((ext_vector_type(2))) unsigned u32x2;
typedef unsigned long long u64;
typedef __attribute__((address_space(1))) u64 gu64;
typedef __attribute__((address_space(3))) u32x4 lds_u32x4;
typedef __attribute__((address_space(3))) u32x2 lds_u32x2;
typedef __attribute__((address_space(3))) f32x4 lds_f32x4;
typedef __attribute__((address_space(3))) f32x2 lds_f32x2;
typedef __attribute__((address_space(3))) char lds_char;

constexpr int S = 2048, D = 640, F = 1728;
constexpr unsigned WS_EPOCH = 0;
constexpr unsigned WS_FLAGW = 4096;
constexpr unsigned WS_FLAG = 32768;
constexpr unsigned WS_FLAGV = 98304;
constexpr unsigned WS_PROF = 131072;
constexpr unsigned WS_W1 = 1u << 18;
constexpr unsigned WS_W2 = WS_W1 + 16 * 160 * 1024;
constexpr unsigned WS_W4 = WS_W2 + 8 * 100 * 1024;
constexpr unsigned WS_W3 = WS_W4 + 8 * 270 * 1024;
constexpr unsigned WS_O = WS_W3 + 108 * 40 * 1024;
constexpr unsigned WS_H = WS_O + 128 * 20 * 1024;
constexpr unsigned WS_A = WS_H + 128 * 20 * 1024;
constexpr unsigned WS_S = WS_A + 128 * 54 * 1024;
constexpr unsigned WS_END = WS_S + 16 * 40 * 1024;
constexpr unsigned SPIN_CAP = 1u << 22;
constexpr int QKV = 40960, W3PRE = 12, CODE_KB = 24;

#define RLX_AGENT __ATOMIC_RELAXED, __HIP_MEMORY_SCOPE_AGENT
#define BAR() asm volatile("s_waitcnt lgkmcnt(0)\n\ts_barrier" ::: "memory")
#define DRAIN() asm volatile("s_waitcnt vmcnt(0)" ::: "memory")
#define CFENCE() do { asm volatile("" ::: "memory"); __builtin_amdgcn_sched_barrier(0); } while (0)
#define MFMA(a, b, c) __builtin_amdgcn_mfma_f32_16x16x32_bf16(__builtin_bit_cast(bf16x8, a), __builtin_bit_cast(bf16x8, b), c, 0, 0, 0)
#define STAMPC(i)
#define STAMP(i)

__device__ __forceinline__ unsigned pk_bf16(float a, float b) {
  f32x2 v = {a, b};
  return __builtin_bit_cast(unsigned, __builtin_convertvector(v, bf16x2));
}
__device__ __forceinline__ float bf_lo(unsigned u) { return __builtin_bit_cast(float, u << 16); }
__device__ __forceinline__ float bf_hi(unsigned u) { return __builtin_bit_cast(float, u & 0xffff0000u); }
__device__ __forceinline__ float dot2(unsigned a, unsigned b, float c) {
  return __builtin_amdgcn_fdot2_f32_bf16(__builtin_bit_cast(bf16x2, a), __builtin_bit_cast(bf16x2, b), c, false);
}
template <int CTRL> __device__ __forceinline__ float dpp(float v) {
  return __builtin_bit_cast(float, __builtin_amdgcn_update_dpp(0, __builtin_bit_cast(int, v), CTRL, 0xf, 0xf, true));
}
__device__ __forceinline__ float sum16(float v) {
  v += dpp<0xB1>(v);
  v += dpp<0x4E>(v);
  v += dpp<0x141>(v);
  v += dpp<0x140>(v);
  return v;
}
__device__ __forceinline__ float sum_rows(float v) {
  unsigned u = __builtin_bit_cast(unsigned, v);
  auto a = __builtin_amdgcn_permlane16_swap(u, u, false, false);
  u = __builtin_bit_cast(unsigned, __builtin_bit_cast(float, (unsigned)a[0]) + __builtin_bit_cast(float, (unsigned)a[1]));
  auto c = __builtin_amdgcn_permlane32_swap(u, u, false, false);
  return __builtin_bit_cast(float, (unsigned)c[0]) + __builtin_bit_cast(float, (unsigned)c[1]);
}
typedef __amdgpu_buffer_rsrc_t rsrc_t;
__device__ __forceinline__ u32x4 ld16(rsrc_t r, unsigned voff, unsigned soff) {
  return __builtin_amdgcn_raw_buffer_load_b128(r, voff, soff, 16);
}
__device__ __forceinline__ void dma16(rsrc_t r, unsigned voff, unsigned soff, unsigned ldsaddr) {
  asm volatile("s_mov_b32 m0, %3\n\ts_nop 0\n\tbuffer_load_dwordx4 %0, %1, %2 offen sc1 lds"
               ::"v"(voff), "s"(r), "s"(soff), "s"(ldsaddr) : "memory");
}
__device__ __forceinline__ void st16(rsrc_t r, u32x4 v, unsigned voff, unsigned soff) {
  __builtin_amdgcn_raw_buffer_store_b128(v, r, voff + soff, 0, 16);
}
__device__ __forceinline__ void st8(rsrc_t r, u32x2 v, unsigned voff, unsigned soff) {
  __builtin_amdgcn_raw_buffer_store_b64(v, r, voff, soff, 16);
}
__device__ __forceinline__ void wait_group(char* ws, int phase, int g, int lane, u64 tag) {
  gu64* f = (gu64*)(ws + WS_FLAG + (unsigned)(phase * 256 + g * 16 + (lane & 15)) * 64u);
#pragma unroll 1
  for (unsigned spins = 0; spins < SPIN_CAP; ++spins) {
    u64 v = __hip_atomic_load(f, RLX_AGENT);
    if (__all(v == tag)) break;
    __builtin_amdgcn_s_sleep(1);
  }
  CFENCE();
}
#define VMWAIT(n) asm volatile("s_waitcnt vmcnt(" #n ")" ::: "memory")
__device__ __forceinline__ void st8_one(rsrc_t r, bool me, u64 v, unsigned off) {
  st8(r, u32x2{(unsigned)v, (unsigned)(v >> 32)}, me ? 0u : 0x80000000u, off);
}
#define raise(phase) st8_one(rs, tid == 448, tag, WS_FLAG + (unsigned)((phase) * 256 + g * 16 + j) * 64u)

__global__ void __launch_bounds__(512, 2)
fused_block(const unsigned short* __restrict__ x, const unsigned short* __restrict__ wqkv,
            const unsigned short* __restrict__ wo, const unsigned short* __restrict__ wg,
            const unsigned short* __restrict__ wu, const unsigned short* __restrict__ wd,
            unsigned short* __restrict__ out, char* ws) {
  __shared__ __attribute__((aligned(16))) char smem_[163840];
  lds_char* smem = (lds_char*)smem_;
  const int tid = threadIdx.x, lane = tid & 63;
  const int w = __builtin_amdgcn_readfirstlane(tid >> 6);
  const int b = blockIdx.x, xcd = b & 7, slot = b >> 3;
  const int g = 4 * (xcd >> 1) + (slot >> 3), j = 8 * (xcd & 1) + (slot & 7), p = j >> 1;
  const int li = lane & 15, lq = lane >> 4;
  const rsrc_t rs = __builtin_amdgcn_make_buffer_rsrc(ws, 0, WS_END, 0x00020000);
  const unsigned lane16 = lane * 16;
  STAMP(0); STAMPC(30);
  const u64 tagv = __hip_atomic_load((gu64*)(ws + WS_EPOCH), RLX_AGENT) + 1;

  const int ftbase = 27 * (p >> 1) + 14 * (p & 1), nft = 14 - (p & 1);
  u32x4 rv[5];
  unsigned rdst[5];
  {
    const int u = (g * 2 + (j & 1)) * 8 + w, kr = lane >> 1, ch = (lane & 1) * 8;
#pragma unroll
    for (int q = 0; q < 5; ++q) {
      const int r = q == 0 ? 0 : q == 1 ? 4 : q - 1;
      int ty, f;
      if (r == 0) { ty = 0; f = u; }
      else if (r == 1) { ty = 3; f = u; }
      else if (r == 2) { ty = 3; f = u + 256; }
      else if (r == 3) { ty = 2; f = u; }
      else {
        ty = u < 64 ? 0 : u < 164 ? 1 : u < 178 ? 2 : 3;
        f = u < 64 ? 256 + u : u < 164 ? u - 64 : u < 178 ? 256 + u - 164 : 512 + u - 178;
        if (ty == 3 && f >= 40 * nft) f = u;
      }
      const unsigned short* src;
      if (ty == 0) {
        const int ks = f >> 4, jj = 2 * p + ((f >> 3) & 1), t = f & 7;
        int c = 16 * t + ch; c = c >= 120 ? 112 : c;
        const int part = c / 40;
        src = wqkv + (32 * ks + kr) * 1920 + part * 600 + 40 * jj + c; rdst[r] = WS_W1 + ((jj * 8 + t) * 20 + ks) * 1024;
      } else if (ty == 1) {
        const int ks = f / 5, t = f - ks * 5, O = 4 * ks + lq, jj = O / 5, o = O - jj * 5, e = kr & 7;
        const int row = 40 * jj + e + (o < 4 ? 20 * (o >> 1) + 8 * (o & 1) : e < 4 ? 16 : 32);
        src = wo + row * 640 + 80 * p + 16 * t + ch; rdst[r] = WS_W2 + (p * 100 + f) * 1024;
      } else if (ty == 2) {
        const int ks = f / 5, t = f - ks * 5;
        src = wd + (32 * ks + kr) * 640 + 80 * p + 16 * t + ch; rdst[r] = WS_W4 + (p * 270 + f) * 1024;
      } else {
        const int ks = (p & 1) ? f / 26 : f / 28, rem = f - ks * 2 * nft, m = rem >= nft, ft = ftbase + rem - m * nft;
        src = (m ? wu : wg) + (32 * ks + kr) * F + 16 * ft + ch; rdst[r] = WS_W3 + ((ft * 2 + m) * 20 + ks) * 1024;
      }
      rv[r] = *(const u32x4*)src;
    }
  }
  CFENCE();
  unsigned code_pf = 0;
  if (w == 7) {
    const unsigned* pc = (const unsigned*)(__builtin_amdgcn_s_getpc() & ~127ull) + lane * 32;
    unsigned t = 0;
#pragma unroll
    for (int i = 0; i < CODE_KB / 8; ++i) t |= __builtin_nontemporal_load(pc + i * 2048) ;
    code_pf = t;
  }
  CFENCE();
  u32x4 xr[5][5];
#pragma unroll
  for (int i = 0; i < 5; ++i) {
    int row = 128 * g - 16 + 4 * (w + 8 * i) + lq;
    row = row < 0 ? 0 : row;
    const u32x4* xp = (const u32x4*)(x + row * D) + li;
    if (i < 4 || w < 4) {
#pragma unroll
      for (int c = 0; c < 5; ++c) xr[i][c] = xp[16 * c];
    }
  }
  CFENCE();
  STAMP(23);
  auto retile = [&](int r) {
    lds_char* sc = smem + 155648 + w * 1024;
    *(lds_u32x4*)(sc + lane16) = rv[r];
    typedef __attribute__((ext_vector_type(4))) short s16x4;
    typedef __attribute__((address_space(3))) s16x4 lds_s16x4;
    const u32x2 lo = __builtin_bit_cast(u32x2, __builtin_amdgcn_ds_read_tr16_b64_v4i16((lds_s16x4*)(sc + lq * 256 + li * 8)));
    const u32x2 hi = __builtin_bit_cast(u32x2, __builtin_amdgcn_ds_read_tr16_b64_v4i16((lds_s16x4*)(sc + lq * 256 + li * 8 + 128)));
    asm volatile("" ::: "memory");
    st16(rs, u32x4{lo[0], lo[1], hi[0], hi[1]}, lane16, rdst[r]);
  };
  retile(0); retile(4);
  CFENCE();
  const u64 tag = ((u64)__builtin_amdgcn_readfirstlane((unsigned)(tagv >> 32)) << 32) | (unsigned)__builtin_amdgcn_readfirstlane((unsigned)tagv);
  STAMP(11);
  asm volatile("" ::"v"(code_pf));
  VMWAIT(0);
  if (lane == 0) __hip_atomic_store((gu64*)(ws + WS_FLAGW + (unsigned)((p * 32 + g * 2 + (j & 1)) * 8 + w) * 8u), tag, RLX_AGENT);
  STAMP(1);
  const unsigned xw = ((li >> 2) * 9 + (w >> 2)) * 1024 + ((li & 3) * 16 + ((4 * (w & 3) + lq) ^ (li & 7))) * 16;
#pragma unroll
  for (int i = 0; i < 5; ++i) {
    if (i < 4 || w < 4) {
      float s1 = 0.f, s2 = 0.f;
#pragma unroll
      for (int c = 0; c < 5; ++c) {
        if (c < 4) *(lds_u32x4*)(smem + xw + (36 * c + 2 * i) * 1024) = xr[i][c];
#pragma unroll
        for (int e = 0; e < 4; ++e) { s1 = dot2(xr[i][c][e], 0x3f803f80u, s1); s2 = dot2(xr[i][c][e], xr[i][c][e], s2); }
      }
      const float mean = sum16(s1) * (1.f / D), var = fmaxf(sum16(s2) * (1.f / D) - mean * mean, 0.f);
      if (li == 0) *(lds_f32x2*)(smem + 147456 + (4 * (w + 8 * i) + lq) * 8) = f32x2{mean, rsqrtf(var + 1e-5f)};
    }
  }
  STAMP(12);
  u32x4 fa = {0, 0, 0, 0}, fc = fa;
  auto pollw = [&](unsigned base) {
#pragma unroll 1
    for (unsigned spins = 0; spins < SPIN_CAP; ++spins) {
      const unsigned tl = (unsigned)tag, th = (unsigned)(tag >> 32);
      bool ok = fa[0] == tl && fa[1] == th && fa[2] == tl && fa[3] == th && fc[0] == tl && fc[1] == th && fc[2] == tl && fc[3] == th;
      if (spins && __all(ok)) break;
      if (spins || base == WS_FLAGW) { fa = ld16(rs, lane16, base + p * 2048); fc = ld16(rs, lane16, base + p * 2048 + 1024); }
      CFENCE();
    }
  };
  if (w == 7) pollw(WS_FLAGW);
  BAR();
  STAMP(2);
  u32x4 w1[20], w2[15], w3[2][20];
  const u32x4 ones = {0x3f803f80u, 0x3f803f80u, 0x3f803f80u, 0x3f803f80u};
  const f32x4 zero4 = {0.f, 0.f, 0.f, 0.f};
  const int ks2 = w < 4 ? 3 * w : 12 + 2 * (w - 4);
  const int ft = ftbase + 7 * (j & 1) + w;
  const bool act3 = w < 7 && 7 * (j & 1) + w < nft;
  const unsigned w3off = WS_W3 + (act3 ? ft : ftbase) * 40 * 1024;
  const unsigned l16a = act3 ? lane16 : 0x80000000u, l16b = w < 4 ? lane16 : 0x80000000u;
#define LDW3(n) w3[(n) & 1][(n) >> 1] = ld16(rs, l16a, w3off + (((n) & 1) * 20 + ((n) >> 1)) * 1024)
  const unsigned w1off = WS_W1 + (j * 8 + w) * 20 * 1024;
#pragma unroll
  for (int k = 0; k < 4; ++k) w1[k] = ld16(rs, lane16 + k * 1024, w1off);
  CFENCE();
  {
    f32x4 acc[9], cs;
    u32x4 af[2][9];
    const unsigned ra0 = (lq * 16 + (li ^ lq)) * 16, ra1 = (lq * 16 + (li ^ (lq + 4))) * 16;
#pragma unroll
    for (int r = 0; r < 9; ++r) af[0][r] = *(lds_u32x4*)(smem + ra0 + r * 1024);
#pragma unroll
    for (int k = 0; k < 20; ++k) {
      if (k == 8) {
        VMWAIT(10);
        st8_one(rs, lane == 0, tag, WS_FLAGV + (unsigned)((p * 32 + g * 2 + (j & 1)) * 8 + w) * 8u);
      }
      if (k == 18) {
        const unsigned l7 = w == 7 ? lane16 : 0x80000000u;
        fa = ld16(rs, l7, WS_FLAGV + p * 2048); fc = ld16(rs, l7, WS_FLAGV + p * 2048 + 1024);
      }
      if (k == 15) { BAR(); STAMP(15); }
      if (k < 16) w1[k + 4] = ld16(rs, lane16 + (k & 3) * 1024, w1off + ((k + 4) & ~3) * 1024);
      if (k >= 1 && k < 16) w2[k - 1] = ld16(rs, k < 11 ? lane16 : l16b, WS_W2 + (p * 100 + ks2 * 5 + k - 1) * 1024);
      CFENCE();
      if (k < 19) {
#pragma unroll
        for (int r = 0; r < 9; ++r) af[(k + 1) & 1][r] = *(lds_u32x4*)(smem + ((k & 1) ? ra0 : ra1) + (((k + 1) & 15) * 9 + r) * 1024);
      }
#pragma unroll
      for (int r = 0; r < 9; ++r) acc[r] = MFMA(w1[k], af[k & 1][r], k == 0 ? zero4 : acc[r]);
      cs = MFMA(w1[k], ones, k == 0 ? zero4 : cs);
#pragma unroll
      for (int r = 0; r < 9; ++r) { __builtin_amdgcn_sched_group_barrier(0x100, 1, 0); __builtin_amdgcn_sched_group_barrier(0x8, 1, 0); }
      CFENCE();
      if (k < 3) { retile(k + 1); CFENCE(); }
      if (k == 3) {
        BAR(); STAMP(14);
#pragma unroll
        for (int i = 0; i < 5; ++i)
          if (i < 4 || w < 4) *(lds_u32x4*)(smem + xw + 2 * i * 1024) = xr[i][4];
      }
    }
    STAMP(16);
    const int c4 = 4 * w + lq, seg = c4 / 5, sub = c4 - 5 * seg;
    lds_char* qb = smem + QKV + li * 368 + (w < 5 ? 80 + (seg & 1) * 200 + (seg >> 1) * 40 + sub * 8 : seg < 6 ? (seg & 1) * 192 + sub * 16 : 160 + sub * 16);
#pragma unroll
    for (int r = 0; r < 9; ++r) {
      const f32x2 st = *(lds_f32x2*)(smem + 147456 + (16 * r + li) * 8);
      const f32x4 y = (acc[r] - st[0] * cs) * st[1];
      if (w < 5) *(lds_u32x2*)(qb + r * 16 * 368) = u32x2{pk_bf16(y[0], y[1]), pk_bf16(y[2], y[3])};
      else *(lds_f32x4*)(qb + r * 16 * 368) = y;
    }
  }
  if (w == 7) pollw(WS_FLAGV);
  BAR();
  STAMP(3);
  const int rtb = 8 * g + 2 * (j & 1);
  const int un[3] = {w, 10 + w, w < 2 ? 8 + w : 16 + (w & 3)};
  u32x2 res[3];
  {
    const int tok = tid >> 2, hd = (tid >> 1) & 1, hf = tid & 1;
    lds_char* base = smem + QKV + (16 + tok) * 368;
    u32x2 qv[5];
#pragma unroll
    for (int c = 0; c < 5; ++c) qv[c] = *(lds_u32x2*)(base + 80 + hd * 200 + c * 8);
    lds_char* kb = base - (hf * 8 + 6) * 368 + hd * 200;
    lds_char* vb = kb - hd * 8;
    float lg[4];
#pragma unroll
    for (int i = 0; i < 4; ++i) {
      float s = 0.f;
#pragma unroll
      for (int c = 0; c < 5; ++c) {
        u32x2 kv = *(lds_u32x2*)(kb + (6 - 2 * i) * 368 + 120 + c * 8);
        s = dot2(qv[c][0], kv[0], s); s = dot2(qv[c][1], kv[1], s);
      }
      lg[i] = (128 * g + tok - 8 * hf - 2 * i >= 0) ? s * 0.22360679774997896f : -1e9f;
      LDW3(3 * i); LDW3(3 * i + 1); LDW3(3 * i + 2);
    }
    { LDW3(12); LDW3(13); LDW3(14); LDW3(15); }
#pragma unroll
    for (int n = 0; n < 3; ++n) {
      const int r = un[n] / 5, t = un[n] - 5 * r;
      res[n] = *(const u32x2*)(x + ((rtb + (r >> 1) * 4 + (r & 1)) * 16 + li) * D + 80 * p + 16 * t + 4 * lq);
    }
    float mx = fmaxf(fmaxf(lg[0], lg[1]), fmaxf(lg[2], lg[3]));
    mx = fmaxf(mx, dpp<0xB1>(mx));
    float sum = 0.f;
#pragma unroll
    for (int i = 0; i < 4; ++i) { lg[i] = __expf(lg[i] - mx); sum += lg[i]; }
    sum += dpp<0xB1>(sum);
    const float inv = __builtin_amdgcn_rcpf(sum);
    f32x4 o[5];
#pragma unroll
    for (int i = 0; i < 4; ++i) {
      const float pr = lg[i] * inv;
#pragma unroll
      for (int c = 0; c < 5; ++c) {
        const f32x4 vv = *(lds_f32x4*)(vb + (6 - 2 * i) * 368 + c * 16);
        o[c] = i == 0 ? pr * vv : o[c] + pr * vv;
      }
    }
    float e8[8], t4[4];
#pragma unroll
    for (int e = 0; e < 8; ++e) {
      const float lo = o[e >> 2][e & 3], hi = o[2 + (e >> 2)][e & 3];
      e8[e] = (hf ? hi : lo) + dpp<0xB1>(hf ? lo : hi);
    }
#pragma unroll
    for (int e = 0; e < 4; ++e) t4[e] = o[4][e] + dpp<0xB1>(o[4][e]);
    const unsigned ob = WS_O + (8 * g + w) * 20 * 1024;
    st16(rs, u32x4{pk_bf16(e8[0], e8[1]), pk_bf16(e8[2], e8[3]), pk_bf16(e8[4], e8[5]), pk_bf16(e8[6], e8[7])},
         (5 * j + 2 * hd + hf) * 256 + (tok & 15) * 16, ob);
    if (!hf) st8(rs, u32x2{pk_bf16(t4[0], t4[1]), pk_bf16(t4[2], t4[3])}, (5 * j + 4) * 256 + (tok & 15) * 16 + hd * 8, ob);
  }
  STAMP(17);
  DRAIN();
  BAR();
  raise(0);
  STAMP(4);
  {
#pragma unroll
    for (int n = 16; n < 2 * W3PRE; ++n) LDW3(n);
  }
  if (w == 7) wait_group(ws, 0, g, lane, tag);
  BAR();
  STAMP(5);
  const unsigned cst = (lq >> 1) * 256 + li * 16 + (lq & 1) * 8;
  {
    u32x4 a[4][3];
#pragma unroll
    for (int r = 0; r < 4; ++r)
#pragma unroll
      for (int k = 0; k < 3; ++k)
        a[r][k] = ld16(rs, (k < 2 ? lane16 : l16b) + k * 1024, WS_O + ((rtb + (r >> 1) * 4 + (r & 1)) * 20 + ks2) * 1024);
    CFENCE();
#pragma unroll
    for (int r = 0; r < 4; ++r) {
      f32x4 acc[5];
#pragma unroll
      for (int t = 0; t < 5; ++t) acc[t] = MFMA(w2[t], a[r][0], zero4);
#pragma unroll
      for (int t = 0; t < 5; ++t) acc[t] = MFMA(w2[5 + t], a[r][1], acc[t]);
      if (w < 4) {
#pragma unroll
        for (int t = 0; t < 5; ++t) acc[t] = MFMA(w2[10 + t], a[r][2], acc[t]);
      }
#pragma unroll
      for (int t = 0; t < 5; ++t) *(lds_f32x4*)(smem + ((r * 5 + t) * 8 + w) * 1024 + lane16) = acc[t];
    }
    STAMP(18);
    BAR();
#pragma unroll
    for (int n = 0; n < 3; ++n) {
      if (n < 2 || w < 4) {
        const int r = un[n] / 5, t = un[n] - 5 * r;
        f32x4 s = {bf_lo(res[n][0]), bf_hi(res[n][0]), bf_lo(res[n][1]), bf_hi(res[n][1])};
#pragma unroll
        for (int i = 0; i < 8; ++i) s += *(lds_f32x4*)(smem + (un[n] * 8 + i) * 1024 + lane16);
        res[n] = u32x2{pk_bf16(s[0], s[1]), pk_bf16(s[2], s[3])};
        st8(rs, res[n], cst, WS_H + (rtb + (r >> 1) * 4 + (r & 1)) * 20 * 1024 + (10 * p + 2 * t) * 256);
        const float s1 = sum_rows(dot2(res[n][0], 0x3f803f80u, dot2(res[n][1], 0x3f803f80u, 0.f)));
        const float s2 = sum_rows(dot2(res[n][0], res[n][0], dot2(res[n][1], res[n][1], 0.f)));
        if (lq == 0) st8(rs, u32x2{__builtin_bit_cast(unsigned, s1), __builtin_bit_cast(unsigned, s2)}, li * 8,
                         WS_S + (g * 40 + 5 * p + t) * 1024 + (2 * (j & 1) + (r >> 1) * 4 + (r & 1)) * 128);
      }
    }
  }
  STAMP(19);
  DRAIN();
  BAR();
  raise(1);
  STAMP(6);
  u32x4 w4[35];
  const int ks4 = w < 6 ? 7 * w : 42 + 6 * (w - 6);
  const unsigned w4off = WS_W4 + (p * 270 + ks4 * 5) * 1024;
  const unsigned lds0 = (unsigned)(size_t)smem, hb = WS_H + 8 * g * 20 * 1024;
#define L16C (w < 6 ? lane16 : 0x80000000u)
#define LDW4(n) do { if ((n) < 35) w4[n] = ld16(rs, (n) < 30 ? lane16 : L16C, w4off + (n) * 1024); } while (0)
#define DMA_A() do { _Pragma("unroll") for (int i = 0; i < 10; ++i) \
    dma16(rs, lane16, hb + ((w & 3) * 20 + 2 * i + (w >> 2)) * 1024, lds0 + (8 * i + w) * 1024); VMWAIT(7); BAR(); } while (0)
#define DMA_B(i) dma16(rs, lane16, hb + ((4 + (w & 3)) * 20 + 2 * (i) + (w >> 2)) * 1024, lds0 + (80 + 8 * (i) + w) * 1024)
  u32x4 a[2][7];
  auto lda = [&](int half) {
#pragma unroll
    for (int r = 0; r < 2; ++r)
#pragma unroll
      for (int k = 0; k < 7; ++k)
        a[r][k] = ld16(rs, (k < 6 ? lane16 : L16C) + (k & 3) * 1024, WS_A + ((rtb + half * 4 + r) * 54 + ks4 + (k & ~3)) * 1024);
    CFENCE();
  };
  if (w == 7) {
    wait_group(ws, 1, g, lane, tag);
    BAR();
    DMA_A();
    f32x4 sv = zero4;
    {
#pragma unroll
      for (int i = 0; i < 9; ++i) DMA_B(i);
      u32x4 sp[40];
#pragma unroll
      for (int n = 0; n < 40; ++n) sp[n] = ld16(rs, lane16 + (n & 3) * 1024, WS_S + (g * 40 + (n & ~3)) * 1024);
      VMWAIT(49); BAR();
      dma16(rs, lane16, hb + 159 * 1024, lds0);
#pragma unroll
      for (int n = 0; n < 40; ++n) sv += __builtin_bit_cast(f32x4, sp[n]);
    }
    const float m0 = sv[0] * (1.f / D), m1 = sv[2] * (1.f / D);
    *(lds_f32x4*)(smem + 162816 + lane16) = f32x4{m0, rsqrtf(fmaxf(sv[1] * (1.f / D) - m0 * m0, 0.f) + 1e-5f),
                                                  m1, rsqrtf(fmaxf(sv[3] * (1.f / D) - m1 * m1, 0.f) + 1e-5f)};
    CFENCE();
#pragma unroll
    for (int n = 0; n < 30; ++n) w3[n & 1][n >> 1] = ld16(rs, lane16, w4off + n * 1024);
    VMWAIT(30); BAR();
    BAR();
    raise(2);
  }
  u32x2 f2 = {0, 0};
#pragma unroll
  for (int n = 0; n < 35; ++n) w4[n] = w3[n & 1][n < 30 ? n >> 1 : 0];
  int wo_ = w;
  asm volatile("" : "+s"(wo_));
  if (wo_ < 7) {
    {
#pragma unroll
      for (int n = 2 * W3PRE; n < 40; ++n) LDW3(n);
    }
    f32x4 cg = zero4, cu = zero4;
#pragma unroll
    for (int k = 0; k < W3PRE; ++k) { cg = MFMA(w3[0][k], ones, cg); cu = MFMA(w3[1][k], ones, cu); }
    CFENCE();
    BAR();
    STAMP(7);
    DMA_A();
    STAMP(13);
    if (w < 4) __builtin_amdgcn_s_setprio(1);
    f32x4 ag[8], au[8];
    u32x4 af[2][4];
    auto epi = [&](int r) {
      const f32x2 st = *(lds_f32x2*)(smem + 162816 + (r * 16 + li) * 8);
      const f32x4 gg = (ag[r] - st[0] * cg) * st[1], uu = (au[r] - st[0] * cu) * st[1];
      float y[4];
#pragma unroll
      for (int e = 0; e < 4; ++e) y[e] = gg[e] * __builtin_amdgcn_rcpf(1.f + __expf(-gg[e])) * uu[e];
      st8(rs, u32x2{pk_bf16(y[0], y[1]), pk_bf16(y[2], y[3])}, cst | (l16a & 0x80000000u), WS_A + (8 * g + r) * 54 * 1024 + ft * 512);
    };
#pragma unroll
    for (int r = 0; r < 4; ++r) af[0][r] = *(lds_u32x4*)(smem + r * 1024 + lane16);
    lds_char* lb = smem + lane16;
    auto halfbody = [&](auto half_) {
     constexpr int h4 = decltype(half_)::value * 4;
#pragma unroll
     for (int k = 0; k < 20; ++k) {
      const int kk = h4 * 5 + k;
      if (kk == 5) { VMWAIT(5); BAR(); }
      if (kk == 18) { VMWAIT(0); BAR(); STAMP(20); }
      if (kk == 26) { VMWAIT(10); BAR(); }
      if (kk < 10) DMA_B(kk);
      if (kk > 20) { LDW4(2 * k - 2); LDW4(2 * k - 1); }
      CFENCE();
      if (kk == 15 || kk == 31) { lb += 65536; asm volatile("" : "+v"(lb)); }
      if (kk < 39) {
#pragma unroll
        for (int r = 0; r < 4; ++r)
          af[(kk + 1) & 1][r] = kk == 38 && r == 3 ? *(lds_u32x4*)(smem + lane16) : *(lds_u32x4*)(lb + (((kk + 1) * 4 + r) & 63) * 1024);
      }
#pragma unroll
      for (int r = 0; r < 4; ++r) {
        ag[h4 + r] = MFMA(w3[0][k], af[kk & 1][r], k == 0 ? zero4 : ag[h4 + r]);
        au[h4 + r] = MFMA(w3[1][k], af[kk & 1][r], k == 0 ? zero4 : au[h4 + r]);
      }
      if (kk >= 5 && kk < 25 - W3PRE) { cg = MFMA(w3[0][(kk + W3PRE - 5) % 20], ones, cg); cu = MFMA(w3[1][(kk + W3PRE - 5) % 20], ones, cu); }
#pragma unroll
      for (int r = 0; r < 4; ++r) { __builtin_amdgcn_sched_group_barrier(0x100, 1, 0); __builtin_amdgcn_sched_group_barrier(0x8, 2, 0); }
      CFENCE();
     }
     if (h4) { STAMP(21); f2 = __builtin_amdgcn_raw_buffer_load_b64(rs, li * 64, WS_FLAG + (2 * 256 + g * 16) * 64, 16); CFENCE(); }
#pragma unroll
     for (int r = 0; r < 4; ++r) { epi(h4 + r); if (r & 1) CFENCE(); }
    };
    halfbody(std::integral_constant<int, 0>{});
    halfbody(std::integral_constant<int, 1>{});
    __builtin_amdgcn_s_setprio(0);
  }
  {
    auto gemm = [&](int half) {
#pragma unroll
      for (int r = 0; r < 2; ++r) {
        f32x4 acc[5];
#pragma unroll
        for (int k = 0; k < 6; ++k)
#pragma unroll
          for (int t = 0; t < 5; ++t) acc[t] = MFMA(w4[k * 5 + t], a[r][k], k == 0 ? zero4 : acc[t]);
        if (w < 6) {
#pragma unroll
          for (int t = 0; t < 5; ++t) acc[t] = MFMA(w4[30 + t], a[r][6], acc[t]);
        }
#pragma unroll
        for (int t = 0; t < 5; ++t) *(lds_f32x4*)(smem + (((half * 2 + r) * 5 + t) * 8 + w) * 1024 + lane16) = acc[t];
      }
      CFENCE();
    };
    auto fin = [&](int n) {
      const int r = un[n] / 5, t = un[n] - 5 * r;
      f32x4 s = {bf_lo(res[n][0]), bf_hi(res[n][0]), bf_lo(res[n][1]), bf_hi(res[n][1])};
#pragma unroll
      for (int i = 0; i < 8; ++i) s += *(lds_f32x4*)(smem + (un[n] * 8 + i) * 1024 + lane16);
      int l2 = lane16;
      asm volatile("" : "+v"(l2));
      *(u32x2*)(out + ((rtb + (r >> 1) * 4 + (r & 1)) * 16 + ((l2 >> 4) & 15)) * D + 80 * p + 16 * t + 4 * (l2 >> 8)) =
          u32x2{pk_bf16(s[0], s[1]), pk_bf16(s[2], s[3])};
    };
    CFENCE();
    if (w == 7 || !__all(f2[0] == (unsigned)tag && f2[1] == (unsigned)(tag >> 32))) wait_group(ws, 2, g, lane, tag);
    CFENCE();
    lda(0);
#pragma unroll
    for (int n = 0; n < 3; ++n) {
      const int r = un[n] / 5, t = un[n] - 5 * r;
      res[n] = __builtin_amdgcn_raw_buffer_load_b64(rs, cst, WS_H + (rtb + (r >> 1) * 4 + (r & 1)) * 20 * 1024 + (10 * p + 2 * t) * 256, 16);
    }
    CFENCE();
    VMWAIT(17);
    BAR();
    raise(3);
    STAMP(8);
    gemm(0);
    if (w == 7) wait_group(ws, 3, g, lane, tag);
    BAR();
    STAMP(9);
    if (b == 0 && tid == 0) __hip_atomic_store((gu64*)(ws + WS_EPOCH), tag, RLX_AGENT);
    lda(1);
    fin(0);
    if (w < 2) fin(2);
    CFENCE();
    gemm(1);
    STAMP(22);
    BAR();
    fin(1);
    if (w == 2 || w == 3) fin(2);
  }
  STAMP(10); STAMPC(31);
}

extern "C" void kernel_launch(void* const* d_in, const int* in_sizes, int n_in, void* d_out, int out_size,
                              void* d_ws, size_t ws_size, hipStream_t stream) {
  fused_block<<<256, 512, 0, stream>>>((const unsigned short*)d_in[0], (const unsigned short*)d_in[1],
                                       (const unsigned short*)d_in[2], (const unsigned short*)d_in[4],
                                       (const unsigned short*)d_in[5], (const unsigned short*)d_in[6],
                                       (unsigned short*)d_out, (char*)d_ws);
}
```

```cpp
#include <hip/hip_runtime.h>
#include <hip/hip_bf16.h>
#include <stdint.h>
#include <type_traits>

typedef __attribute__((ext_vector_type(8))) __bf16 bf16x8;
typedef __attribute__((ext_vector_type(2))) __bf16 bf16x2;
typedef __attribute__((ext_vector_type(4))) float f32x4;
typedef __attribute__((ext_vector_type(2))) float f32x2;
typedef __attribute__((ext_vector_type(4))) unsigned u32x4;
typedef __attribute__((ext_vector_type(2))) unsigned u32x2;
typedef unsigned long long u64;
typedef __attribute__((address_space(1))) u64 gu64;
typedef __attribute__((address_space(3))) u32x4 lds_u32x4;
typedef __attribute__((address_space(3))) u32x2 lds_u32x2;
typedef __attribute__((address_space(3))) f32x4 lds_f32x4;
typedef __attribute__((address_space(3))) f32x2 lds_f32x2;
typedef __attribute__((address_space(3))) char lds_char;

constexpr int S = 2048, D = 640, F = 1728;
constexpr unsigned WS_EPOCH = 0;
constexpr unsigned WS_FLAGW = 4096;
constexpr unsigned WS_FLAG = 32768;
constexpr unsigned WS_FLAGV = 98304;
constexpr unsigned WS_PROF = 131072;
constexpr unsigned WS_W1 = 1u << 18;
constexpr unsigned WS_W2 = WS_W1 + 16 * 160 * 1024;
constexpr unsigned WS_W4 = WS_W2 + 8 * 100 * 1024;
constexpr unsigned WS_W3 = WS_W4 + 8 * 270 * 1024;
constexpr unsigned WS_O = WS_W3 + 108 * 40 * 1024;
constexpr unsigned WS_H = WS_O + 128 * 20 * 1024;
constexpr unsigned WS_A = WS_H + 128 * 20 * 1024;
constexpr unsigned WS_S = WS_A + 128 * 54 * 1024;
constexpr unsigned WS_END = WS_S + 16 * 40 * 1024;
constexpr unsigned SPIN_CAP = 1u << 22;
constexpr int QKV = 40960, W3PRE = 12;

#define RLX_AGENT __ATOMIC_RELAXED, __HIP_MEMORY_SCOPE_AGENT
#define BAR() asm volatile("s_waitcnt lgkmcnt(0)\n\ts_barrier" ::: "memory")
#define DRAIN() asm volatile("s_waitcnt vmcnt(0)" ::: "memory")
#define CFENCE() do { asm volatile("" ::: "memory"); __builtin_amdgcn_sched_barrier(0); } while (0)
#define MFMA(a, b, c) __builtin_amdgcn_mfma_f32_16x16x32_bf16(__builtin_bit_cast(bf16x8, a), __builtin_bit_cast(bf16x8, b), c, 0, 0, 0)
#define STAMPC(i)
#define STAMP(i)

__device__ __forceinline__ unsigned pk_bf16(float a, float b) {
  f32x2 v = {a, b};
  return __builtin_bit_cast(unsigned, __builtin_convertvector(v, bf16x2));
}
__device__ __forceinline__ float bf_lo(unsigned u) { return __builtin_bit_cast(float, u << 16); }
__device__ __forceinline__ float bf_hi(unsigned u) { return __builtin_bit_cast(float, u & 0xffff0000u); }
__device__ __forceinline__ float dot2(unsigned a, unsigned b, float c) {
  return __builtin_amdgcn_fdot2_f32_bf16(__builtin_bit_cast(bf16x2, a), __builtin_bit_cast(bf16x2, b), c, false);
}
template <int CTRL> __device__ __forceinline__ float dpp(float v) {
  return __builtin_bit_cast(float, __builtin_amdgcn_update_dpp(0, __builtin_bit_cast(int, v), CTRL, 0xf, 0xf, true));
}
__device__ __forceinline__ float sum16(float v) {
  v += dpp<0xB1>(v);
  v += dpp<0x4E>(v);
  v += dpp<0x141>(v);
  v += dpp<0x140>(v);
  return v;
}
__device__ __forceinline__ float sum_rows(float v) {
  unsigned u = __builtin_bit_cast(unsigned, v);
  auto a = __builtin_amdgcn_permlane16_swap(u, u, false, false);
  u = __builtin_bit_cast(unsigned, __builtin_bit_cast(float, (unsigned)a[0]) + __builtin_bit_cast(float, (unsigned)a[1]));
  auto c = __builtin_amdgcn_permlane32_swap(u, u, false, false);
  return __builtin_bit_cast(float, (unsigned)c[0]) + __builtin_bit_cast(float, (unsigned)c[1]);
}
typedef __amdgpu_buffer_rsrc_t rsrc_t;
__device__ __forceinline__ u32x4 ld16(rsrc_t r, unsigned voff, unsigned soff) {
  return __builtin_amdgcn_raw_buffer_load_b128(r, voff, soff, 16);
}
__device__ __forceinline__ void dma16(rsrc_t r, unsigned voff, unsigned soff, unsigned ldsaddr) {
  asm volatile("s_mov_b32 m0, %3\n\ts_nop 0\n\tbuffer_load_dwordx4 %0, %1, %2 offen sc1 lds"
               ::"v"(voff), "s"(r), "s"(soff), "s"(ldsaddr) : "memory");
}
__device__ __forceinline__ void st16(rsrc_t r, u32x4 v, unsigned voff, unsigned soff) {
  __builtin_amdgcn_raw_buffer_store_b128(v, r, voff + soff, 0, 16);
}
__device__ __forceinline__ void st8(rsrc_t r, u32x2 v, unsigned voff, unsigned soff) {
  __builtin_amdgcn_raw_buffer_store_b64(v, r, voff, soff, 16);
}
__device__ __forceinline__ void wait_group(char* ws, int phase, int g, int lane, u64 tag) {
  gu64* f = (gu64*)(ws + WS_FLAG + (unsigned)(phase * 256 + g * 16 + (lane & 15)) * 64u);
#pragma unroll 1
  for (unsigned spins = 0; spins < SPIN_CAP; ++spins) {
    u64 v = __hip_atomic_load(f, RLX_AGENT);
    if (__all(v == tag)) break;
    __builtin_amdgcn_s_sleep(1);
  }
  CFENCE();
}
#define VMWAIT(n) asm volatile("s_waitcnt vmcnt(" #n ")" ::: "memory")
__device__ __forceinline__ void st8_one(rsrc_t r, bool me, u64 v, unsigned off) {
  st8(r, u32x2{(unsigned)v, (unsigned)(v >> 32)}, me ? 0u : 0x80000000u, off);
}
#define raise(phase) st8_one(rs, tid == 448, tag, WS_FLAG + (unsigned)((phase) * 256 + g * 16 + j) * 64u)

__global__ void __launch_bounds__(512, 2)
fused_block(const unsigned short* __restrict__ x, const unsigned short* __restrict__ wqkv,
            const unsigned short* __restrict__ wo, const unsigned short* __restrict__ wg,
            const unsigned short* __restrict__ wu, const unsigned short* __restrict__ wd,
            unsigned short* __restrict__ out, char* ws) {
  __shared__ __attribute__((aligned(16))) char smem_[163840];
  lds_char* smem = (lds_char*)smem_;
  const int tid = threadIdx.x, lane = tid & 63;
  const int w = __builtin_amdgcn_readfirstlane(tid >> 6);
  const int b = blockIdx.x, xcd = b & 7, slot = b >> 3;
  const int g = 4 * (xcd >> 1) + (slot >> 3), j = 8 * (xcd & 1) + (slot & 7), p = j >> 1;
  const int li = lane & 15, lq = lane >> 4;
  const rsrc_t rs = __builtin_amdgcn_make_buffer_rsrc(ws, 0, WS_END, 0x00020000);
  const unsigned lane16 = lane * 16;
  STAMP(0); STAMPC(30);
  const u64 tagv = __hip_atomic_load((gu64*)(ws + WS_EPOCH), RLX_AGENT) + 1;

  const int ftbase = 27 * (p >> 1) + 14 * (p & 1), nft = 14 - (p & 1);
  u32x4 rv[5];
  unsigned rdst[5];
  auto ldw = [&](int r) {
    const int u = (g * 2 + (j & 1)) * 8 + w, kr = lane >> 1, ch = (lane & 1) * 8;
    {
      int ty, f;
      if (r == 0) { ty = 0; f = u; }
      else if (r == 1) { ty = 3; f = u; }
      else if (r == 2) { ty = 3; f = u + 256; }
      else if (r == 3) { ty = 2; f = u; }
      else {
        ty = u < 64 ? 0 : u < 164 ? 1 : u < 178 ? 2 : 3;
        f = u < 64 ? 256 + u : u < 164 ? u - 64 : u < 178 ? 256 + u - 164 : 512 + u - 178;
        if (ty == 3 && f >= 40 * nft) f = u;
      }
      const unsigned short* src;
      if (ty == 0) {
        const int ks = f >> 4, jj = 2 * p + ((f >> 3) & 1), t = f & 7;
        int c = 16 * t + ch; c = c >= 120 ? 112 : c;
        const int part = c / 40;
        src = wqkv + (32 * ks + kr) * 1920 + part * 600 + 40 * jj + c; rdst[r] = WS_W1 + ((jj * 8 + t) * 20 + ks) * 1024;
      } else if (ty == 1) {
        const int ks = f / 5, t = f - ks * 5, O = 4 * ks + lq, jj = O / 5, o = O - jj * 5, e = kr & 7;
        const int row = 40 * jj + e + (o < 4 ? 20 * (o >> 1) + 8 * (o & 1) : e < 4 ? 16 : 32);
        src = wo + row * 640 + 80 * p + 16 * t + ch; rdst[r] = WS_W2 + (p * 100 + f) * 1024;
      } else if (ty == 2) {
        const int ks = f / 5, t = f - ks * 5;
        src = wd + (32 * ks + kr) * 640 + 80 * p + 16 * t + ch; rdst[r] = WS_W4 + (p * 270 + f) * 1024;
      } else {
        const int ks = (p & 1) ? f / 26 : f / 28, rem = f - ks * 2 * nft, m = rem >= nft, ft = ftbase + rem - m * nft;
        src = (m ? wu : wg) + (32 * ks + kr) * F + 16 * ft + ch; rdst[r] = WS_W3 + ((ft * 2 + m) * 20 + ks) * 1024;
      }
      rv[r] = *(const u32x4*)src;
    }
  };
  ldw(0); ldw(4);
  CFENCE();
  u32x4 xr[5][5];
  auto ldx = [&](int i) {
    int row = 128 * g - 16 + 4 * (w + 8 * i) + lq;
    row = row < 0 ? 0 : row;
    const u32x4* xp = (const u32x4*)(x + row * D) + li;
    if (i < 4 || w < 4) {
#pragma unroll
      for (int c = 0; c < 5; ++c) xr[i][c] = xp[16 * c];
    }
  };
  ldx(0); ldx(1);
  CFENCE();
  unsigned code_pf[4] = {0, 0, 0, 0};
  unsigned tlb[3];
  {
    const unsigned ob5 = w == 5 ? 0u : 0x80000000u, stp = (WS_END / 128) & ~127u;
    tlb[0] = __builtin_amdgcn_raw_buffer_load_b32(rs, lane * stp | ob5, 0, 16);
    tlb[1] = __builtin_amdgcn_raw_buffer_load_b32(rs, lane * stp | ob5, 64 * stp, 16);
    tlb[2] = __builtin_amdgcn_raw_buffer_load_b32(__builtin_amdgcn_make_buffer_rsrc(out, 0, 2048 * D * 2, 0x00020000), lane * 40960 | ob5, 0, 16);
  }
  if (w == 6) {
    typedef __attribute__((address_space(1))) const unsigned gcu32;
    gcu32* pc = (gcu32*)(__builtin_amdgcn_s_getpc() & ~127ull);
#pragma unroll
    for (int i = 0; i < 4; ++i) code_pf[i] = pc[i * 2048 + (i < 3 || lane < 48 ? lane : 47) * 32];
  }
  CFENCE();
  STAMP(23);
  auto retile = [&](int r) {
    lds_char* sc = smem + 155648 + w * 1024;
    *(lds_u32x4*)(sc + lane16) = rv[r];
    typedef __attribute__((ext_vector_type(4))) short s16x4;
    typedef __attribute__((address_space(3))) s16x4 lds_s16x4;
    const u32x2 lo = __builtin_bit_cast(u32x2, __builtin_amdgcn_ds_read_tr16_b64_v4i16((lds_s16x4*)(sc + lq * 256 + li * 8)));
    const u32x2 hi = __builtin_bit_cast(u32x2, __builtin_amdgcn_ds_read_tr16_b64_v4i16((lds_s16x4*)(sc + lq * 256 + li * 8 + 128)));
    asm volatile("" ::: "memory");
    st16(rs, u32x4{lo[0], lo[1], hi[0], hi[1]}, lane16, rdst[r]);
  };
  retile(0); retile(4);
  CFENCE();
  ldx(2); ldx(3); ldw(1); ldw(2); ldw(3);
  CFENCE();
  const u64 tag = ((u64)__builtin_amdgcn_readfirstlane((unsigned)(tagv >> 32)) << 32) | (unsigned)__builtin_amdgcn_readfirstlane((unsigned)tagv);
  STAMP(11);
  VMWAIT(13);
  if (lane == 0) __hip_atomic_store((gu64*)(ws + WS_FLAGW + (unsigned)((p * 32 + g * 2 + (j & 1)) * 8 + w) * 8u), tag, RLX_AGENT);
  STAMP(1);
  CFENCE();
  ldx(4);
  CFENCE();
  const unsigned xw = ((li >> 2) * 9 + (w >> 2)) * 1024 + ((li & 3) * 16 + ((4 * (w & 3) + lq) ^ (li & 7))) * 16;
#pragma unroll
  for (int i = 0; i < 5; ++i) {
    if (i < 4 || w < 4) {
      float s1 = 0.f, s2 = 0.f;
#pragma unroll
      for (int c = 0; c < 5; ++c) {
        if (c < 4) *(lds_u32x4*)(smem + xw + (36 * c + 2 * i) * 1024) = xr[i][c];
#pragma unroll
        for (int e = 0; e < 4; ++e) { s1 = dot2(xr[i][c][e], 0x3f803f80u, s1); s2 = dot2(xr[i][c][e], xr[i][c][e], s2); }
      }
      const float mean = sum16(s1) * (1.f / D), var = fmaxf(sum16(s2) * (1.f / D) - mean * mean, 0.f);
      if (li == 0) *(lds_f32x2*)(smem + 147456 + (4 * (w + 8 * i) + lq) * 8) = f32x2{mean, rsqrtf(var + 1e-5f)};
    }
  }
  STAMP(12);
  u32x4 fa = {0, 0, 0, 0}, fc = fa;
  auto pollw = [&](unsigned base) {
#pragma unroll 1
    for (unsigned spins = 0; spins < SPIN_CAP; ++spins) {
      const unsigned tl = (unsigned)tag, th = (unsigned)(tag >> 32);
      bool ok = fa[0] == tl && fa[1] == th && fa[2] == tl && fa[3] == th && fc[0] == tl && fc[1] == th && fc[2] == tl && fc[3] == th;
      if (spins && __all(ok)) break;
      { fa = ld16(rs, lane16, base + p * 2048); fc = ld16(rs, lane16, base + p * 2048 + 1024); }
      CFENCE();
    }
  };
  if (w == 7) pollw(WS_FLAGW);
  BAR();
  STAMP(2);
  u32x4 w1[20], w2[15], w3[2][20];
  const u32x4 ones = {0x3f803f80u, 0x3f803f80u, 0x3f803f80u, 0x3f803f80u};
  const f32x4 zero4 = {0.f, 0.f, 0.f, 0.f};
  const int ks2 = w < 4 ? 3 * w : 12 + 2 * (w - 4);
  const int ft = ftbase + 7 * (j & 1) + w;
  const bool act3 = w < 7 && 7 * (j & 1) + w < nft;
  const unsigned w3off = WS_W3 + (act3 ? ft : ftbase) * 40 * 1024;
  const unsigned l16a = act3 ? lane16 : 0x80000000u, l16b = w < 4 ? lane16 : 0x80000000u;
#define LDW3(n) w3[(n) & 1][(n) >> 1] = ld16(rs, l16a, w3off + (((n) & 1) * 20 + ((n) >> 1)) * 1024)
  const unsigned w1off = WS_W1 + (j * 8 + w) * 20 * 1024;
#pragma unroll
  for (int k = 0; k < 8; ++k) w1[k] = ld16(rs, lane16 + (k & 3) * 1024, w1off + (k & 4) * 1024);
  CFENCE();
  {
    f32x4 acc[9], cs;
    u32x4 af[2][9];
    const unsigned ra0 = (lq * 16 + (li ^ lq)) * 16, ra1 = (lq * 16 + (li ^ (lq + 4))) * 16;
#pragma unroll
    for (int r = 0; r < 9; ++r) af[0][r] = *(lds_u32x4*)(smem + ra0 + r * 1024);
#pragma unroll
    for (int k = 0; k < 20; ++k) {
      if (k == 15) { BAR(); STAMP(15); }
      if (k < 12) w1[k + 8] = ld16(rs, lane16 + (k & 3) * 1024, w1off + ((k + 8) & ~3) * 1024);
      if (k >= 5) w2[k - 5] = ld16(rs, k < 15 ? lane16 : l16b, WS_W2 + (p * 100 + ks2 * 5 + k - 5) * 1024);
      CFENCE();
      if (k < 19) {
        lds_char* pb = smem + ((k & 1) ? ra0 : ra1) + (((k + 1) & 15) / 7) * 64512;
        asm("" : "+v"(pb));
#pragma unroll
        for (int r = 0; r < 9; ++r) af[(k + 1) & 1][r] = *(lds_u32x4*)(pb + ((((k + 1) & 15) % 7) * 9 + r) * 1024);
      }
#pragma unroll
      for (int r = 0; r < 9; ++r) acc[r] = MFMA(w1[k], af[k & 1][r], k == 0 ? zero4 : acc[r]);
      cs = MFMA(w1[k], ones, k == 0 ? zero4 : cs);
#pragma unroll
      for (int r = 0; r < 9; ++r) { __builtin_amdgcn_sched_group_barrier(0x100, 1, 0); __builtin_amdgcn_sched_group_barrier(0x8, 1, 0); }
      CFENCE();
      if (k >= 12 && k < 15) { retile(k - 11); CFENCE(); }
      if (k == 3) {
        BAR(); STAMP(14);
  asm volatile("" ::"v"(code_pf[0]), "v"(code_pf[1]), "v"(code_pf[2]), "v"(code_pf[3]), "v"(tlb[0]), "v"(tlb[1]), "v"(tlb[2]));
#pragma unroll
        for (int i = 0; i < 5; ++i)
          if (i < 4 || w < 4) *(lds_u32x4*)(smem + xw + 2 * i * 1024) = xr[i][4];
      }
    }
    STAMP(16);
    const int c4 = 4 * w + lq, seg = c4 / 5, sub = c4 - 5 * seg;
    lds_char* qb = smem + QKV + li * 368 + (w < 5 ? 80 + (seg & 1) * 200 + (seg >> 1) * 40 + sub * 8 : seg < 6 ? (seg & 1) * 192 + sub * 16 : 160 + sub * 16);
#pragma unroll
    for (int r = 0; r < 9; ++r) {
      const f32x2 st = *(lds_f32x2*)(smem + 147456 + (16 * r + li) * 8);
      const f32x4 y = (acc[r] - st[0] * cs) * st[1];
      if (w < 5) *(lds_u32x2*)(qb + r * 16 * 368) = u32x2{pk_bf16(y[0], y[1]), pk_bf16(y[2], y[3])};
      else *(lds_f32x4*)(qb + r * 16 * 368) = y;
    }
  }
  VMWAIT(5);
  st8_one(rs, lane == 0, tag, WS_FLAGV + (unsigned)((p * 32 + g * 2 + (j & 1)) * 8 + w) * 8u);
  BAR();
  STAMP(3);
  const int rtb = 8 * g + 2 * (j & 1);
  const int un[3] = {w, 10 + w, w < 2 ? 8 + w : 16 + (w & 3)};
  u32x2 res[3];
  {
    const int tok = tid >> 2, hd = (tid >> 1) & 1, hf = tid & 1;
    lds_char* base = smem + QKV + (16 + tok) * 368;
    u32x2 qv[5];
#pragma unroll
    for (int c = 0; c < 5; ++c) qv[c] = *(lds_u32x2*)(base + 80 + hd * 200 + c * 8);
    lds_char* kb = base - (hf * 8 + 6) * 368 + hd * 200;
    lds_char* vb = kb - hd * 8;
    float lg[4];
#pragma unroll
    for (int i = 0; i < 4; ++i) {
      float s = 0.f;
#pragma unroll
      for (int c = 0; c < 5; ++c) {
        u32x2 kv = *(lds_u32x2*)(kb + (6 - 2 * i) * 368 + 120 + c * 8);
        s = dot2(qv[c][0], kv[0], s); s = dot2(qv[c][1], kv[1], s);
      }
      lg[i] = (128 * g + tok - 8 * hf - 2 * i >= 0) ? s * 0.22360679774997896f : -1e9f;
    }
#pragma unroll
    for (int n = 0; n < 3; ++n) {
      const int r = un[n] / 5, t = un[n] - 5 * r;
      res[n] = *(const u32x2*)(x + ((rtb + (r >> 1) * 4 + (r & 1)) * 16 + li) * D + 80 * p + 16 * t + 4 * lq);
    }
    float mx = fmaxf(fmaxf(lg[0], lg[1]), fmaxf(lg[2], lg[3]));
    mx = fmaxf(mx, dpp<0xB1>(mx));
    float sum = 0.f;
#pragma unroll
    for (int i = 0; i < 4; ++i) { lg[i] = __expf(lg[i] - mx); sum += lg[i]; }
    sum += dpp<0xB1>(sum);
    const float inv = __builtin_amdgcn_rcpf(sum);
    f32x4 o[5];
#pragma unroll
    for (int i = 0; i < 4; ++i) {
      const float pr = lg[i] * inv;
#pragma unroll
      for (int c = 0; c < 5; ++c) {
        const f32x4 vv = *(lds_f32x4*)(vb + (6 - 2 * i) * 368 + c * 16);
        o[c] = i == 0 ? pr * vv : o[c] + pr * vv;
      }
    }
    float e8[8], t4[4];
#pragma unroll
    for (int e = 0; e < 8; ++e) {
      const float lo = o[e >> 2][e & 3], hi = o[2 + (e >> 2)][e & 3];
      e8[e] = (hf ? hi : lo) + dpp<0xB1>(hf ? lo : hi);
    }
#pragma unroll
    for (int e = 0; e < 4; ++e) t4[e] = o[4][e] + dpp<0xB1>(o[4][e]);
    const unsigned ob = WS_O + (8 * g + w) * 20 * 1024;
    st16(rs, u32x4{pk_bf16(e8[0], e8[1]), pk_bf16(e8[2], e8[3]), pk_bf16(e8[4], e8[5]), pk_bf16(e8[6], e8[7])},
         (5 * j + 2 * hd + hf) * 256 + (tok & 15) * 16, ob);
    if (!hf) st8(rs, u32x2{pk_bf16(t4[0], t4[1]), pk_bf16(t4[2], t4[3])}, (5 * j + 4) * 256 + (tok & 15) * 16 + hd * 8, ob);
  }
  STAMP(17);
  if (w == 7) pollw(WS_FLAGV);
  DRAIN();
  BAR();
  raise(0);
  STAMP(4);
  {
#pragma unroll
    for (int n = 0; n < 12; ++n) LDW3(n);
  }
  if (w == 7) wait_group(ws, 0, g, lane, tag);
  BAR();
  STAMP(5);
  const unsigned cst = (lq >> 1) * 256 + li * 16 + (lq & 1) * 8;
  {
    u32x4 a[4][3];
#pragma unroll
    for (int r = 0; r < 4; ++r)
#pragma unroll
      for (int k = 0; k < 3; ++k)
        a[r][k] = ld16(rs, (k < 2 ? lane16 : l16b) + k * 1024, WS_O + ((rtb + (r >> 1) * 4 + (r & 1)) * 20 + ks2) * 1024);
    CFENCE();
#pragma unroll
    for (int r = 0; r < 4; ++r) {
      f32x4 acc[5];
#pragma unroll
      for (int t = 0; t < 5; ++t) acc[t] = MFMA(w2[t], a[r][0], zero4);
#pragma unroll
      for (int t = 0; t < 5; ++t) acc[t] = MFMA(w2[5 + t], a[r][1], acc[t]);
      if (w < 4) {
#pragma unroll
        for (int t = 0; t < 5; ++t) acc[t] = MFMA(w2[10 + t], a[r][2], acc[t]);
      }
#pragma unroll
      for (int t = 0; t < 5; ++t) *(lds_f32x4*)(smem + ((r * 5 + t) * 8 + w) * 1024 + lane16) = acc[t];
    }
    STAMP(18);
    CFENCE();
#pragma unroll
    for (int n = 12; n < 2 * W3PRE; ++n) LDW3(n);
    BAR();
#pragma unroll
    for (int n = 0; n < 3; ++n) {
      if (n < 2 || w < 4) {
        const int r = un[n] / 5, t = un[n] - 5 * r;
        f32x4 s = {bf_lo(res[n][0]), bf_hi(res[n][0]), bf_lo(res[n][1]), bf_hi(res[n][1])};
#pragma unroll
        for (int i = 0; i < 8; ++i) s += *(lds_f32x4*)(smem + (un[n] * 8 + i) * 1024 + lane16);
        res[n] = u32x2{pk_bf16(s[0], s[1]), pk_bf16(s[2], s[3])};
        st8(rs, res[n], cst, WS_H + (rtb + (r >> 1) * 4 + (r & 1)) * 20 * 1024 + (10 * p + 2 * t) * 256);
        const float s1 = sum_rows(dot2(res[n][0], 0x3f803f80u, dot2(res[n][1], 0x3f803f80u, 0.f)));
        const float s2 = sum_rows(dot2(res[n][0], res[n][0], dot2(res[n][1], res[n][1], 0.f)));
        if (lq == 0) st8(rs, u32x2{__builtin_bit_cast(unsigned, s1), __builtin_bit_cast(unsigned, s2)}, li * 8,
                         WS_S + (g * 40 + 5 * p + t) * 1024 + (2 * (j & 1) + (r >> 1) * 4 + (r & 1)) * 128);
      }
    }
  }
  STAMP(19);
  DRAIN();
  BAR();
  raise(1);
  STAMP(6);
  u32x4 w4[35];
  const int ks4 = w < 6 ? 7 * w : 42 + 6 * (w - 6);
  const unsigned w4off = WS_W4 + (p * 270 + ks4 * 5) * 1024;
  const unsigned lds0 = (unsigned)(size_t)smem, hb = WS_H + 8 * g * 20 * 1024;
#define L16C (w < 6 ? lane16 : 0x80000000u)
#define LDW4(n) do { if ((n) < 35) w4[n] = ld16(rs, (n) < 30 ? lane16 : L16C, w4off + (n) * 1024); } while (0)
#define DMA_A() do { _Pragma("unroll") for (int i = 0; i < 10; ++i) \
    dma16(rs, lane16, hb + ((w & 3) * 20 + 2 * i + (w >> 2)) * 1024, lds0 + (8 * i + w) * 1024); VMWAIT(7); BAR(); } while (0)
#define DMA_B(i) dma16(rs, lane16, hb + ((4 + (w & 3)) * 20 + 2 * (i) + (w >> 2)) * 1024, lds0 + (80 + 8 * (i) + w) * 1024)
  u32x4 a[2][7];
  auto lda = [&](int half) {
#pragma unroll
    for (int r = 0; r < 2; ++r)
#pragma unroll
      for (int k = 0; k < 7; ++k)
        a[r][k] = ld16(rs, (k < 6 ? lane16 : L16C) + (k & 3) * 1024, WS_A + ((rtb + half * 4 + r) * 54 + ks4 + (k & ~3)) * 1024);
    CFENCE();
  };
  if (w == 7) {
    wait_group(ws, 1, g, lane, tag);
    BAR();
    DMA_A();
    f32x4 sv = zero4;
    {
#pragma unroll
      for (int i = 0; i < 9; ++i) DMA_B(i);
      u32x4 sp[40];
#pragma unroll
      for (int n = 0; n < 40; ++n) sp[n] = ld16(rs, lane16 + (n & 3) * 1024, WS_S + (g * 40 + (n & ~3)) * 1024);
      VMWAIT(49); BAR();
      dma16(rs, lane16, hb + 159 * 1024, lds0);
#pragma unroll
      for (int n = 0; n < 40; ++n) sv += __builtin_bit_cast(f32x4, sp[n]);
    }
    const float m0 = sv[0] * (1.f / D), m1 = sv[2] * (1.f / D);
    *(lds_f32x4*)(smem + 162816 + lane16) = f32x4{m0, rsqrtf(fmaxf(sv[1] * (1.f / D) - m0 * m0, 0.f) + 1e-5f),
                                                  m1, rsqrtf(fmaxf(sv[3] * (1.f / D) - m1 * m1, 0.f) + 1e-5f)};
    CFENCE();
#pragma unroll
    for (int n = 0; n < 30; ++n) w3[n & 1][n >> 1] = ld16(rs, lane16, w4off + n * 1024);
    VMWAIT(30); BAR();
    BAR();
    raise(2);
  }
  u32x2 f2 = {0, 0};
#pragma unroll
  for (int n = 0; n < 35; ++n) w4[n] = w3[n & 1][n < 30 ? n >> 1 : 0];
  int wo_ = w;
  asm volatile("" : "+s"(wo_));
  if (wo_ < 7) {
    {
#pragma unroll
      for (int n = 2 * W3PRE; n < 40; ++n) LDW3(n);
    }
    f32x4 cg = zero4, cu = zero4;
#pragma unroll
    for (int k = 0; k < W3PRE; ++k) { cg = MFMA(w3[0][k], ones, cg); cu = MFMA(w3[1][k], ones, cu); }
    CFENCE();
    BAR();
    STAMP(7);
    DMA_A();
    STAMP(13);
    if (w < 4) __builtin_amdgcn_s_setprio(1);
    f32x4 ag[8], au[8];
    u32x4 af[2][4];
    auto epi = [&](int r) {
      const f32x2 st = *(lds_f32x2*)(smem + 162816 + (r * 16 + li) * 8);
      const f32x4 gg = (ag[r] - st[0] * cg) * st[1], uu = (au[r] - st[0] * cu) * st[1];
      float y[4];
#pragma unroll
      for (int e = 0; e < 4; ++e) y[e] = gg[e] * __builtin_amdgcn_rcpf(1.f + __expf(-gg[e])) * uu[e];
      st8(rs, u32x2{pk_bf16(y[0], y[1]), pk_bf16(y[2], y[3])}, cst | (l16a & 0x80000000u), WS_A + (8 * g + r) * 54 * 1024 + ft * 512);
    };
#pragma unroll
    for (int r = 0; r < 4; ++r) af[0][r] = *(lds_u32x4*)(smem + r * 1024 + lane16);
    lds_char* lb = smem + lane16;
    auto halfbody = [&](auto half_) {
     constexpr int h4 = decltype(half_)::value * 4;
#pragma unroll
     for (int k = 0; k < 20; ++k) {
      const int kk = h4 * 5 + k;
      if (kk == 5) { VMWAIT(5); BAR(); }
      if (kk == 18) { VMWAIT(0); BAR(); STAMP(20); }
      if (kk == 26) { VMWAIT(10); BAR(); }
      if (kk < 10) DMA_B(kk);
      if (kk > 20) { LDW4(2 * k - 2); LDW4(2 * k - 1); }
      CFENCE();
      if (kk == 15 || kk == 31) { lb += 65536; asm volatile("" : "+v"(lb)); }
      if (kk < 39) {
#pragma unroll
        for (int r = 0; r < 4; ++r)
          af[(kk + 1) & 1][r] = kk == 38 && r == 3 ? *(lds_u32x4*)(smem + lane16) : *(lds_u32x4*)(lb + (((kk + 1) * 4 + r) & 63) * 1024);
      }
#pragma unroll
      for (int r = 0; r < 4; ++r) {
        ag[h4 + r] = MFMA(w3[0][k], af[kk & 1][r], k == 0 ? zero4 : ag[h4 + r]);
        au[h4 + r] = MFMA(w3[1][k], af[kk & 1][r], k == 0 ? zero4 : au[h4 + r]);
      }
      if (kk >= 5 && kk < 25 - W3PRE) { cg = MFMA(w3[0][(kk + W3PRE - 5) % 20], ones, cg); cu = MFMA(w3[1][(kk + W3PRE - 5) % 20], ones, cu); }
#pragma unroll
      for (int r = 0; r < 4; ++r) { __builtin_amdgcn_sched_group_barrier(0x100, 1, 0); __builtin_amdgcn_sched_group_barrier(0x8, 2, 0); }
      CFENCE();
     }
     if (h4) { STAMP(21); f2 = __builtin_amdgcn_raw_buffer_load_b64(rs, li * 64, WS_FLAG + (2 * 256 + g * 16) * 64, 16); CFENCE(); }
#pragma unroll
     for (int r = 0; r < 4; ++r) { epi(h4 + r); if (r & 1) CFENCE(); }
    };
    halfbody(std::integral_constant<int, 0>{});
    halfbody(std::integral_constant<int, 1>{});
    __builtin_amdgcn_s_setprio(0);
  }
  {
    auto gemm = [&](int half) {
#pragma unroll
      for (int r = 0; r < 2; ++r) {
        f32x4 acc[5];
#pragma unroll
        for (int k = 0; k < 6; ++k)
#pragma unroll
          for (int t = 0; t < 5; ++t) acc[t] = MFMA(w4[k * 5 + t], a[r][k], k == 0 ? zero4 : acc[t]);
        if (w < 6) {
#pragma unroll
          for (int t = 0; t < 5; ++t) acc[t] = MFMA(w4[30 + t], a[r][6], acc[t]);
        }
#pragma unroll
        for (int t = 0; t < 5; ++t) *(lds_f32x4*)(smem + (((half * 2 + r) * 5 + t) * 8 + w) * 1024 + lane16) = acc[t];
      }
      CFENCE();
    };
    auto fin = [&](int n) {
      const int r = un[n] / 5, t = un[n] - 5 * r;
      f32x4 s = {bf_lo(res[n][0]), bf_hi(res[n][0]), bf_lo(res[n][1]), bf_hi(res[n][1])};
#pragma unroll
      for (int i = 0; i < 8; ++i) s += *(lds_f32x4*)(smem + (un[n] * 8 + i) * 1024 + lane16);
      int l2 = lane16;
      asm volatile("" : "+v"(l2));
      *(u32x2*)(out + ((rtb + (r >> 1) * 4 + (r & 1)) * 16 + ((l2 >> 4) & 15)) * D + 80 * p + 16 * t + 4 * (l2 >> 8)) =
          u32x2{pk_bf16(s[0], s[1]), pk_bf16(s[2], s[3])};
    };
    CFENCE();
    if (w == 7 || !__all(f2[0] == (unsigned)tag && f2[1] == (unsigned)(tag >> 32))) wait_group(ws, 2, g, lane, tag);
    CFENCE();
    lda(0);
#pragma unroll
    for (int n = 0; n < 3; ++n) {
      const int r = un[n] / 5, t = un[n] - 5 * r;
      res[n] = __builtin_amdgcn_raw_buffer_load_b64(rs, cst, WS_H + (rtb + (r >> 1) * 4 + (r & 1)) * 20 * 1024 + (10 * p + 2 * t) * 256, 16);
    }
    CFENCE();
    VMWAIT(17);
    BAR();
    raise(3);
    STAMP(8);
    gemm(0);
    if (w == 7) wait_group(ws, 3, g, lane, tag);
    BAR();
    STAMP(9);
    if (b == 0 && tid == 0) __hip_atomic_store((gu64*)(ws + WS_EPOCH), tag, RLX_AGENT);
    lda(1);
    fin(0);
    if (w < 2) fin(2);
    CFENCE();
    gemm(1);
    STAMP(22);
    BAR();
    fin(1);
    if (w == 2 || w == 3) fin(2);
  }
  STAMP(10); STAMPC(31);
}

extern "C" void kernel_launch(void* const* d_in, const int* in_sizes, int n_in, void* d_out, int out_size,
                              void* d_ws, size_t ws_size, hipStream_t stream) {
  fused_block<<<256, 512, 0, stream>>>((const unsigned short*)d_in[0], (const unsigned short*)d_in[1],
                                       (const unsigned short*)d_in[2], (const unsigned short*)d_in[4],
                                       (const unsigned short*)d_in[5], (const unsigned short*)d_in[6],
                                       (unsigned short*)d_out, (char*)d_ws);
}
```

```cpp
#include <hip/hip_runtime.h>
#include <hip/hip_bf16.h>
#include <stdint.h>
#include <type_traits>

typedef __attribute__((ext_vector_type(8))) __bf16 bf16x8;
typedef __attribute__((ext_vector_type(2))) __bf16 bf16x2;
typedef __attribute__((ext_vector_type(4))) float f32x4;
typedef __attribute__((ext_vector_type(2))) float f32x2;
typedef __attribute__((ext_vector_type(4))) unsigned u32x4;
typedef __attribute__((ext_vector_type(2))) unsigned u32x2;
typedef unsigned long long u64;
typedef __attribute__((address_space(1))) u64 gu64;
typedef __attribute__((address_space(3))) u32x4 lds_u32x4;
typedef __attribute__((address_space(3))) u32x2 lds_u32x2;
typedef __attribute__((address_space(3))) f32x4 lds_f32x4;
typedef __attribute__((address_space(3))) f32x2 lds_f32x2;
typedef __attribute__((address_space(3))) char lds_char;

constexpr int S = 2048, D = 640, F = 1728;
constexpr unsigned WS_EPOCH = 0;
constexpr unsigned WS_FLAGW = 4096;
constexpr unsigned WS_FLAG = 32768;
constexpr unsigned WS_FLAGV = 98304;
constexpr unsigned WS_PROF = 131072;
constexpr unsigned WS_W1 = 1u << 18;
constexpr unsigned WS_W2 = WS_W1 + 16 * 160 * 1024;
constexpr unsigned WS_W4 = WS_W2 + 8 * 100 * 1024;
constexpr unsigned WS_W3 = WS_W4 + 8 * 270 * 1024;
constexpr unsigned WS_O = WS_W3 + 108 * 40 * 1024;
constexpr unsigned WS_H = WS_O + 128 * 20 * 1024;
constexpr unsigned WS_A = WS_H + 128 * 20 * 1024;
constexpr unsigned WS_S = WS_A + 128 * 54 * 1024;
constexpr unsigned WS_END = WS_S + 16 * 40 * 1024;
constexpr unsigned SPIN_CAP = 1u << 22;
constexpr int QKV = 40960, W3PRE = 12;

#define RLX_AGENT __ATOMIC_RELAXED, __HIP_MEMORY_SCOPE_AGENT
#define BAR() asm volatile("s_waitcnt lgkmcnt(0)\n\ts_barrier" ::: "memory")
#define DRAIN() asm volatile("s_waitcnt vmcnt(0)" ::: "memory")
#define CFENCE() do { asm volatile("" ::: "memory"); __builtin_amdgcn_sched_barrier(0); } while (0)
#define MFMA(a, b, c) __builtin_amdgcn_mfma_f32_16x16x32_bf16(__builtin_bit_cast(bf16x8, a), __builtin_bit_cast(bf16x8, b), c, 0, 0, 0)
#define STAMPC(i)
#define STAMP(i)

__device__ __forceinline__ unsigned pk_bf16(float a, float b) {
  f32x2 v = {a, b};
  return __builtin_bit_cast(unsigned, __builtin_convertvector(v, bf16x2));
}
__device__ __forceinline__ float bf_lo(unsigned u) { return __builtin_bit_cast(float, u << 16); }
__device__ __forceinline__ float bf_hi(unsigned u) { return __builtin_bit_cast(float, u & 0xffff0000u); }
__device__ __forceinline__ float dot2(unsigned a, unsigned b, float c) {
  return __builtin_amdgcn_fdot2_f32_bf16(__builtin_bit_cast(bf16x2, a), __builtin_bit_cast(bf16x2, b), c, false);
}
template <int CTRL> __device__ __forceinline__ float dpp(float v) {
  return __builtin_bit_cast(float, __builtin_amdgcn_update_dpp(0, __builtin_bit_cast(int, v), CTRL, 0xf, 0xf, true));
}
__device__ __forceinline__ float sum16(float v) {
  v += dpp<0xB1>(v);
  v += dpp<0x4E>(v);
  v += dpp<0x141>(v);
  v += dpp<0x140>(v);
  return v;
}
__device__ __forceinline__ float sum_rows(float v) {
  unsigned u = __builtin_bit_cast(unsigned, v);
  auto a = __builtin_amdgcn_permlane16_swap(u, u, false, false);
  u = __builtin_bit_cast(unsigned, __builtin_bit_cast(float, (unsigned)a[0]) + __builtin_bit_cast(float, (unsigned)a[1]));
  auto c = __builtin_amdgcn_permlane32_swap(u, u, false, false);
  return __builtin_bit_cast(float, (unsigned)c[0]) + __builtin_bit_cast(float, (unsigned)c[1]);
}
typedef __amdgpu_buffer_rsrc_t rsrc_t;
__device__ __forceinline__ u32x4 ld16(rsrc_t r, unsigned voff, unsigned soff) {
  return __builtin_amdgcn_raw_buffer_load_b128(r, voff, soff, 16);
}
__device__ __forceinline__ void dma16(rsrc_t r, unsigned voff, unsigned soff, unsigned ldsaddr) {
  asm volatile("s_mov_b32 m0, %3\n\ts_nop 0\n\tbuffer_load_dwordx4 %0, %1, %2 offen sc1 lds"
               ::"v"(voff), "s"(r), "s"(soff), "s"(ldsaddr) : "memory");
}
__device__ __forceinline__ void st16(rsrc_t r, u32x4 v, unsigned voff, unsigned soff) {
  __builtin_amdgcn_raw_buffer_store_b128(v, r, voff + soff, 0, 16);
}
__device__ __forceinline__ void st8(rsrc_t r, u32x2 v, unsigned voff, unsigned soff) {
  __builtin_amdgcn_raw_buffer_store_b64(v, r, voff, soff, 16);
}
__device__ __forceinline__ void wait_group(char* ws, int phase, int g, int lane, u64 tag) {
  gu64* f = (gu64*)(ws + WS_FLAG + (unsigned)(phase * 256 + g * 16 + (lane & 15)) * 64u);
#pragma unroll 1
  for (unsigned spins = 0; spins < SPIN_CAP; ++spins) {
    u64 v = __hip_atomic_load(f, RLX_AGENT);
    if (__all(v == tag)) break;
    __builtin_amdgcn_s_sleep(1);
  }
  CFENCE();
}
#define VMWAIT(n) asm volatile("s_waitcnt vmcnt(" #n ")" ::: "memory")
__device__ __forceinline__ void st8_one(rsrc_t r, bool me, u64 v, unsigned off) {
  st8(r, u32x2{(unsigned)v, (unsigned)(v >> 32)}, me ? 0u : 0x80000000u, off);
}
#define raise(phase) st8_one(rs, tid == 448, tag, WS_FLAG + (unsigned)((phase) * 256 + g * 16 + j) * 64u)

__global__ void __launch_bounds__(512, 2)
fused_block(const unsigned short* __restrict__ x, const unsigned short* __restrict__ wqkv,
            const unsigned short* __restrict__ wo, const unsigned short* __restrict__ wg,
            const unsigned short* __restrict__ wu, const unsigned short* __restrict__ wd,
            unsigned short* __restrict__ out, char* ws) {
  __shared__ __attribute__((aligned(16))) char smem_[163840];
  lds_char* smem = (lds_char*)smem_;
  const int tid = threadIdx.x, lane = tid & 63;
  const int w = __builtin_amdgcn_readfirstlane(tid >> 6);
  const int b = blockIdx.x, xcd = b & 7, slot = b >> 3;
  const int g = 4 * (xcd >> 1) + (slot >> 3), j = 8 * (xcd & 1) + (slot & 7), p = j >> 1;
  const int li = lane & 15, lq = lane >> 4;
  const rsrc_t rs = __builtin_amdgcn_make_buffer_rsrc(ws, 0, WS_END, 0x00020000);
  const unsigned lane16 = lane * 16;
  STAMP(0); STAMPC(30);
  const u64 tagv = __hip_atomic_load((gu64*)(ws + WS_EPOCH), RLX_AGENT) + 1;

  const int ftbase = 27 * (p >> 1) + 14 * (p & 1), nft = 14 - (p & 1);
  u32x4 rv[5];
  unsigned rdst[5];
  auto ldw = [&](int r) {
    const int u = (g * 2 + (j & 1)) * 8 + w, kr = lane >> 1, ch = (lane & 1) * 8;
    {
      int ty, f;
      if (r == 0) { ty = 0; f = u; }
      else if (r == 1) { ty = 3; f = u; }
      else if (r == 2) { ty = 3; f = u + 256; }
      else if (r == 3) { ty = 2; f = u; }
      else {
        ty = u < 64 ? 0 : u < 164 ? 1 : u < 178 ? 2 : 3;
        f = u < 64 ? 256 + u : u < 164 ? u - 64 : u < 178 ? 256 + u - 164 : 512 + u - 178;
        if (ty == 3 && f >= 40 * nft) f = u;
      }
      const unsigned short* src;
      if (ty == 0) {
        const int ks = f >> 4, jj = 2 * p + ((f >> 3) & 1), t = f & 7;
        int c = 16 * t + ch; c = c >= 120 ? 112 : c;
        const int part = c / 40;
        src = wqkv + (32 * ks + kr) * 1920 + part * 600 + 40 * jj + c; rdst[r] = WS_W1 + ((jj * 8 + t) * 20 + ks) * 1024;
      } else if (ty == 1) {
        const int ks = f / 5, t = f - ks * 5, O = 4 * ks + lq, jj = O / 5, o = O - jj * 5, e = kr & 7;
        const int row = 40 * jj + e + (o < 4 ? 20 * (o >> 1) + 8 * (o & 1) : e < 4 ? 16 : 32);
        src = wo + row * 640 + 80 * p + 16 * t + ch; rdst[r] = WS_W2 + (p * 100 + f) * 1024;
      } else if (ty == 2) {
        const int ks = f / 5, t = f - ks * 5;
        src = wd + (32 * ks + kr) * 640 + 80 * p + 16 * t + ch; rdst[r] = WS_W4 + (p * 270 + f) * 1024;
      } else {
        const int ks = (p & 1) ? f / 26 : f / 28, rem = f - ks * 2 * nft, m = rem >= nft, ft = ftbase + rem - m * nft;
        src = (m ? wu : wg) + (32 * ks + kr) * F + 16 * ft + ch; rdst[r] = WS_W3 + ((ft * 2 + m) * 20 + ks) * 1024;
      }
      rv[r] = *(const u32x4*)src;
    }
  };
  ldw(0); ldw(4);
  CFENCE();
  u32x4 xr[5][5];
  auto ldx = [&](int i) {
    int row = 128 * g - 16 + 4 * (w + 8 * i) + lq;
    row = row < 0 ? 0 : row;
    const u32x4* xp = (const u32x4*)(x + row * D) + li;
    if (i < 4 || w < 4) {
#pragma unroll
      for (int c = 0; c < 5; ++c) xr[i][c] = xp[16 * c];
    }
  };
  ldx(0); ldx(1);
  CFENCE();
  unsigned code_pf[4] = {0, 0, 0, 0};
  if (w == 6) {
    typedef __attribute__((address_space(1))) const unsigned gcu32;
    gcu32* pc = (gcu32*)(__builtin_amdgcn_s_getpc() & ~127ull);
#pragma unroll
    for (int i = 0; i < 4; ++i) code_pf[i] = pc[i * 2048 + (i < 3 || lane < 48 ? lane : 47) * 32];
  }
  CFENCE();
  STAMP(23);
  auto retile = [&](int r) {
    lds_char* sc = smem + 155648 + w * 1024;
    *(lds_u32x4*)(sc + lane16) = rv[r];
    typedef __attribute__((ext_vector_type(4))) short s16x4;
    typedef __attribute__((address_space(3))) s16x4 lds_s16x4;
    const u32x2 lo = __builtin_bit_cast(u32x2, __builtin_amdgcn_ds_read_tr16_b64_v4i16((lds_s16x4*)(sc + lq * 256 + li * 8)));
    const u32x2 hi = __builtin_bit_cast(u32x2, __builtin_amdgcn_ds_read_tr16_b64_v4i16((lds_s16x4*)(sc + lq * 256 + li * 8 + 128)));
    asm volatile("" ::: "memory");
    st16(rs, u32x4{lo[0], lo[1], hi[0], hi[1]}, lane16, rdst[r]);
  };
  retile(0); retile(4);
  CFENCE();
  ldx(2); ldx(3); ldw(1); ldw(2); ldw(3);
  CFENCE();
  const u64 tag = ((u64)__builtin_amdgcn_readfirstlane((unsigned)(tagv >> 32)) << 32) | (unsigned)__builtin_amdgcn_readfirstlane((unsigned)tagv);
  STAMP(11);
  VMWAIT(13);
  if (lane == 0) __hip_atomic_store((gu64*)(ws + WS_FLAGW + (unsigned)((p * 32 + g * 2 + (j & 1)) * 8 + w) * 8u), tag, RLX_AGENT);
  STAMP(1);
  CFENCE();
  ldx(4);
  CFENCE();
  const unsigned xw = ((li >> 2) * 9 + (w >> 2)) * 1024 + ((li & 3) * 16 + ((4 * (w & 3) + lq) ^ (li & 7))) * 16;
#pragma unroll
  for (int i = 0; i < 5; ++i) {
    if (i < 4 || w < 4) {
      float s1 = 0.f, s2 = 0.f;
#pragma unroll
      for (int c = 0; c < 5; ++c) {
        if (c < 4) *(lds_u32x4*)(smem + xw + (36 * c + 2 * i) * 1024) = xr[i][c];
#pragma unroll
        for (int e = 0; e < 4; ++e) { s1 = dot2(xr[i][c][e], 0x3f803f80u, s1); s2 = dot2(xr[i][c][e], xr[i][c][e], s2); }
      }
      const float mean = sum16(s1) * (1.f / D), var = fmaxf(sum16(s2) * (1.f / D) - mean * mean, 0.f);
      if (li == 0) *(lds_f32x2*)(smem + 147456 + (4 * (w + 8 * i) + lq) * 8) = f32x2{mean, rsqrtf(var + 1e-5f)};
    }
  }
  STAMP(12);
  u32x4 fa = {0, 0, 0, 0}, fc = fa;
  auto pollw = [&](unsigned base) {
#pragma unroll 1
    for (unsigned spins = 0; spins < SPIN_CAP; ++spins) {
      const unsigned tl = (unsigned)tag, th = (unsigned)(tag >> 32);
      bool ok = fa[0] == tl && fa[1] == th && fa[2] == tl && fa[3] == th && fc[0] == tl && fc[1] == th && fc[2] == tl && fc[3] == th;
      if (spins && __all(ok)) break;
      { fa = ld16(rs, lane16, base + p * 2048); fc = ld16(rs, lane16, base + p * 2048 + 1024); }
      CFENCE();
    }
  };
  if (w == 7) pollw(WS_FLAGW);
  BAR();
  STAMP(2);
  u32x4 w1[20], w2[15], w3[2][20];
  const u32x4 ones = {0x3f803f80u, 0x3f803f80u, 0x3f803f80u, 0x3f803f80u};
  const f32x4 zero4 = {0.f, 0.f, 0.f, 0.f};
  const int ks2 = w < 4 ? 3 * w : 12 + 2 * (w - 4);
  const int ft = ftbase + 7 * (j & 1) + w;
  const bool act3 = w < 7 && 7 * (j & 1) + w < nft;
  const unsigned w3off = WS_W3 + (act3 ? ft : ftbase) * 40 * 1024;
  const unsigned l16a = act3 ? lane16 : 0x80000000u, l16b = w < 4 ? lane16 : 0x80000000u;
#define LDW3(n) w3[(n) & 1][(n) >> 1] = ld16(rs, l16a, w3off + (((n) & 1) * 20 + ((n) >> 1)) * 1024)
  const unsigned w1off = WS_W1 + (j * 8 + w) * 20 * 1024;
#pragma unroll
  for (int k = 0; k < 8; ++k) w1[k] = ld16(rs, lane16 + (k & 3) * 1024, w1off + (k & 4) * 1024);
  CFENCE();
  {
    f32x4 acc[9], cs;
    u32x4 af[2][9];
    const unsigned ra0 = (lq * 16 + (li ^ lq)) * 16, ra1 = (lq * 16 + (li ^ (lq + 4))) * 16;
#pragma unroll
    for (int r = 0; r < 9; ++r) af[0][r] = *(lds_u32x4*)(smem + ra0 + r * 1024);
#pragma unroll
    for (int k = 0; k < 20; ++k) {
      if (k == 15) { BAR(); STAMP(15); }
      if (k < 12) w1[k + 8] = ld16(rs, lane16 + (k & 3) * 1024, w1off + ((k + 8) & ~3) * 1024);
      if (k >= 5) w2[k - 5] = ld16(rs, k < 15 ? lane16 : l16b, WS_W2 + (p * 100 + ks2 * 5 + k - 5) * 1024);
      CFENCE();
      if (k < 19) {
        lds_char* pb = smem + ((k & 1) ? ra0 : ra1) + (((k + 1) & 15) / 7) * 64512;
        asm("" : "+v"(pb));
#pragma unroll
        for (int r = 0; r < 9; ++r) af[(k + 1) & 1][r] = *(lds_u32x4*)(pb + ((((k + 1) & 15) % 7) * 9 + r) * 1024);
      }
#pragma unroll
      for (int r = 0; r < 9; ++r) acc[r] = MFMA(w1[k], af[k & 1][r], k == 0 ? zero4 : acc[r]);
      cs = MFMA(w1[k], ones, k == 0 ? zero4 : cs);
#pragma unroll
      for (int r = 0; r < 9; ++r) { __builtin_amdgcn_sched_group_barrier(0x100, 1, 0); __builtin_amdgcn_sched_group_barrier(0x8, 1, 0); }
      CFENCE();
      if (k >= 12 && k < 15) { retile(k - 11); CFENCE(); }
      if (k == 3) {
        BAR(); STAMP(14);
  asm volatile("" ::"v"(code_pf[0]), "v"(code_pf[1]), "v"(code_pf[2]), "v"(code_pf[3]));
#pragma unroll
        for (int i = 0; i < 5; ++i)
          if (i < 4 || w < 4) *(lds_u32x4*)(smem + xw + 2 * i * 1024) = xr[i][4];
      }
    }
    STAMP(16);
    const int c4 = 4 * w + lq, seg = c4 / 5, sub = c4 - 5 * seg;
    lds_char* qb = smem + QKV + li * 368 + (w < 5 ? 80 + (seg & 1) * 200 + (seg >> 1) * 40 + sub * 8 : seg < 6 ? (seg & 1) * 192 + sub * 16 : 160 + sub * 16);
#pragma unroll
    for (int r = 0; r < 9; ++r) {
      const f32x2 st = *(lds_f32x2*)(smem + 147456 + (16 * r + li) * 8);
      const f32x4 y = (acc[r] - st[0] * cs) * st[1];
      if (w < 5) *(lds_u32x2*)(qb + r * 16 * 368) = u32x2{pk_bf16(y[0], y[1]), pk_bf16(y[2], y[3])};
      else *(lds_f32x4*)(qb + r * 16 * 368) = y;
    }
  }
  VMWAIT(5);
  st8_one(rs, lane == 0, tag, WS_FLAGV + (unsigned)((p * 32 + g * 2 + (j & 1)) * 8 + w) * 8u);
  BAR();
  STAMP(3);
  const int rtb = 8 * g + 2 * (j & 1);
  const int un[3] = {w, 10 + w, w < 2 ? 8 + w : 16 + (w & 3)};
  u32x2 res[3];
  {
    const int tok = tid >> 2, hd = (tid >> 1) & 1, hf = tid & 1;
    lds_char* base = smem + QKV + (16 + tok) * 368;
    u32x2 qv[5];
#pragma unroll
    for (int c = 0; c < 5; ++c) qv[c] = *(lds_u32x2*)(base + 80 + hd * 200 + c * 8);
    lds_char* kb = base - (hf * 8 + 6) * 368 + hd * 200;
    lds_char* vb = kb - hd * 8;
    float lg[4];
#pragma unroll
    for (int i = 0; i < 4; ++i) {
      float s = 0.f;
#pragma unroll
      for (int c = 0; c < 5; ++c) {
        u32x2 kv = *(lds_u32x2*)(kb + (6 - 2 * i) * 368 + 120 + c * 8);
        s = dot2(qv[c][0], kv[0], s); s = dot2(qv[c][1], kv[1], s);
      }
      lg[i] = (128 * g + tok - 8 * hf - 2 * i >= 0) ? s * 0.22360679774997896f : -1e9f;
    }
#pragma unroll
    for (int n = 0; n < 3; ++n) {
      const int r = un[n] / 5, t = un[n] - 5 * r;
      res[n] = *(const u32x2*)(x + ((rtb + (r >> 1) * 4 + (r & 1)) * 16 + li) * D + 80 * p + 16 * t + 4 * lq);
    }
    float mx = fmaxf(fmaxf(lg[0], lg[1]), fmaxf(lg[2], lg[3]));
    mx = fmaxf(mx, dpp<0xB1>(mx));
    float sum = 0.f;
#pragma unroll
    for (int i = 0; i < 4; ++i) { lg[i] = __expf(lg[i] - mx); sum += lg[i]; }
    sum += dpp<0xB1>(sum);
    const float inv = __builtin_amdgcn_rcpf(sum);
    f32x4 o[5];
#pragma unroll
    for (int i = 0; i < 4; ++i) {
      const float pr = lg[i] * inv;
#pragma unroll
      for (int c = 0; c < 5; ++c) {
        const f32x4 vv = *(lds_f32x4*)(vb + (6 - 2 * i) * 368 + c * 16);
        o[c] = i == 0 ? pr * vv : o[c] + pr * vv;
      }
    }
    float e8[8], t4[4];
#pragma unroll
    for (int e = 0; e < 8; ++e) {
      const float lo = o[e >> 2][e & 3], hi = o[2 + (e >> 2)][e & 3];
      e8[e] = (hf ? hi : lo) + dpp<0xB1>(hf ? lo : hi);
    }
#pragma unroll
    for (int e = 0; e < 4; ++e) t4[e] = o[4][e] + dpp<0xB1>(o[4][e]);
    const unsigned ob = WS_O + (8 * g + w) * 20 * 1024;
    st16(rs, u32x4{pk_bf16(e8[0], e8[1]), pk_bf16(e8[2], e8[3]), pk_bf16(e8[4], e8[5]), pk_bf16(e8[6], e8[7])},
         (5 * j + 2 * hd + hf) * 256 + (tok & 15) * 16, ob);
    if (!hf) st8(rs, u32x2{pk_bf16(t4[0], t4[1]), pk_bf16(t4[2], t4[3])}, (5 * j + 4) * 256 + (tok & 15) * 16 + hd * 8, ob);
  }
  STAMP(17);
  if (w == 7) pollw(WS_FLAGV);
  DRAIN();
  BAR();
  raise(0);
  STAMP(4);
  {
#pragma unroll
    for (int n = 0; n < 12; ++n) LDW3(n);
  }
  if (w == 7) wait_group(ws, 0, g, lane, tag);
  BAR();
  STAMP(5);
  const unsigned cst = (lq >> 1) * 256 + li * 16 + (lq & 1) * 8;
  {
    u32x4 a[4][3];
#pragma unroll
    for (int r = 0; r < 4; ++r)
#pragma unroll
      for (int k = 0; k < 3; ++k)
        a[r][k] = ld16(rs, (k < 2 ? lane16 : l16b) + k * 1024, WS_O + ((rtb + (r >> 1) * 4 + (r & 1)) * 20 + ks2) * 1024);
    CFENCE();
#pragma unroll
    for (int r = 0; r < 4; ++r) {
      f32x4 acc[5];
#pragma unroll
      for (int t = 0; t < 5; ++t) acc[t] = MFMA(w2[t], a[r][0], zero4);
#pragma unroll
      for (int t = 0; t < 5; ++t) acc[t] = MFMA(w2[5 + t], a[r][1], acc[t]);
      if (w < 4) {
#pragma unroll
        for (int t = 0; t < 5; ++t) acc[t] = MFMA(w2[10 + t], a[r][2], acc[t]);
      }
#pragma unroll
      for (int t = 0; t < 5; ++t) *(lds_f32x4*)(smem + ((r * 5 + t) * 8 + w) * 1024 + lane16) = acc[t];
    }
    STAMP(18);
    CFENCE();
#pragma unroll
    for (int n = 12; n < 2 * W3PRE; ++n) LDW3(n);
    BAR();
#pragma unroll
    for (int n = 0; n < 3; ++n) {
      if (n < 2 || w < 4) {
        const int r = un[n] / 5, t = un[n] - 5 * r;
        f32x4 s = {bf_lo(res[n][0]), bf_hi(res[n][0]), bf_lo(res[n][1]), bf_hi(res[n][1])};
#pragma unroll
        for (int i = 0; i < 8; ++i) s += *(lds_f32x4*)(smem + (un[n] * 8 + i) * 1024 + lane16);
        res[n] = u32x2{pk_bf16(s[0], s[1]), pk_bf16(s[2], s[3])};
        st8(rs, res[n], cst, WS_H + (rtb + (r >> 1) * 4 + (r & 1)) * 20 * 1024 + (10 * p + 2 * t) * 256);
        const float s1 = sum_rows(dot2(res[n][0], 0x3f803f80u, dot2(res[n][1], 0x3f803f80u, 0.f)));
        const float s2 = sum_rows(dot2(res[n][0], res[n][0], dot2(res[n][1], res[n][1], 0.f)));
        if (lq == 0) st8(rs, u32x2{__builtin_bit_cast(unsigned, s1), __builtin_bit_cast(unsigned, s2)}, li * 8,
                         WS_S + (g * 40 + 5 * p + t) * 1024 + (2 * (j & 1) + (r >> 1) * 4 + (r & 1)) * 128);
      }
    }
  }
  STAMP(19);
  DRAIN();
  BAR();
  raise(1);
  STAMP(6);
  u32x4 w4[35];
  const int ks4 = w < 6 ? 7 * w : 42 + 6 * (w - 6);
  const unsigned w4off = WS_W4 + (p * 270 + ks4 * 5) * 1024;
  const unsigned lds0 = (unsigned)(size_t)smem, hb = WS_H + 8 * g * 20 * 1024;
#define L16C (w < 6 ? lane16 : 0x80000000u)
#define LDW4(n) do { if ((n) < 35) w4[n] = ld16(rs, (n) < 30 ? lane16 : L16C, w4off + (n) * 1024); } while (0)
#define DMA_A() do { _Pragma("unroll") for (int i = 0; i < 10; ++i) \
    dma16(rs, lane16, hb + ((w & 3) * 20 + 2 * i + (w >> 2)) * 1024, lds0 + (8 * i + w) * 1024); VMWAIT(7); BAR(); } while (0)
#define DMA_B(i) dma16(rs, lane16, hb + ((4 + (w & 3)) * 20 + 2 * (i) + (w >> 2)) * 1024, lds0 + (80 + 8 * (i) + w) * 1024)
  u32x4 a[2][7];
  auto lda = [&](int half) {
#pragma unroll
    for (int r = 0; r < 2; ++r)
#pragma unroll
      for (int k = 0; k < 7; ++k)
        a[r][k] = ld16(rs, (k < 6 ? lane16 : L16C) + (k & 3) * 1024, WS_A + ((rtb + half * 4 + r) * 54 + ks4 + (k & ~3)) * 1024);
    CFENCE();
  };
  if (w == 7) {
    wait_group(ws, 1, g, lane, tag);
    BAR();
    DMA_A();
    f32x4 sv = zero4;
    {
#pragma unroll
      for (int i = 0; i < 9; ++i) DMA_B(i);
      u32x4 sp[40];
#pragma unroll
      for (int n = 0; n < 40; ++n) sp[n] = ld16(rs, lane16 + (n & 3) * 1024, WS_S + (g * 40 + (n & ~3)) * 1024);
      VMWAIT(49); BAR();
      dma16(rs, lane16, hb + 159 * 1024, lds0);
#pragma unroll
      for (int n = 0; n < 40; ++n) sv += __builtin_bit_cast(f32x4, sp[n]);
    }
    const float m0 = sv[0] * (1.f / D), m1 = sv[2] * (1.f / D);
    *(lds_f32x4*)(smem + 162816 + lane16) = f32x4{m0, rsqrtf(fmaxf(sv[1] * (1.f / D) - m0 * m0, 0.f) + 1e-5f),
                                                  m1, rsqrtf(fmaxf(sv[3] * (1.f / D) - m1 * m1, 0.f) + 1e-5f)};
    CFENCE();
#pragma unroll
    for (int n = 0; n < 30; ++n) w3[n & 1][n >> 1] = ld16(rs, lane16, w4off + n * 1024);
    VMWAIT(30); BAR();
    BAR();
    raise(2);
  }
  u32x2 f2 = {0, 0};
#pragma unroll
  for (int n = 0; n < 35; ++n) w4[n] = w3[n & 1][n < 30 ? n >> 1 : 0];
  int wo_ = w;
  asm volatile("" : "+s"(wo_));
  if (wo_ < 7) {
    {
#pragma unroll
      for (int n = 2 * W3PRE; n < 40; ++n) LDW3(n);
    }
    f32x4 cg = zero4, cu = zero4;
#pragma unroll
    for (int k = 0; k < W3PRE; ++k) { cg = MFMA(w3[0][k], ones, cg); cu = MFMA(w3[1][k], ones, cu); }
    CFENCE();
    BAR();
    STAMP(7);
    DMA_A();
    STAMP(13);
    if (w < 4) __builtin_amdgcn_s_setprio(1);
    f32x4 ag[8], au[8];
    u32x4 af[2][4];
    auto epi = [&](int r) {
      const f32x2 st = *(lds_f32x2*)(smem + 162816 + (r * 16 + li) * 8);
      const f32x4 gg = (ag[r] - st[0] * cg) * st[1], uu = (au[r] - st[0] * cu) * st[1];
      float y[4];
#pragma unroll
      for (int e = 0; e < 4; ++e) y[e] = gg[e] * __builtin_amdgcn_rcpf(1.f + __expf(-gg[e])) * uu[e];
      st8(rs, u32x2{pk_bf16(y[0], y[1]), pk_bf16(y[2], y[3])}, cst | (l16a & 0x80000000u), WS_A + (8 * g + r) * 54 * 1024 + ft * 512);
    };
#pragma unroll
    for (int r = 0; r < 4; ++r) af[0][r] = *(lds_u32x4*)(smem + r * 1024 + lane16);
    lds_char* lb = smem + lane16;
    auto halfbody = [&](auto half_) {
     constexpr int h4 = decltype(half_)::value * 4;
#pragma unroll
     for (int k = 0; k < 20; ++k) {
      const int kk = h4 * 5 + k;
      if (kk == 5) { VMWAIT(5); BAR(); }
      if (kk == 18) { VMWAIT(0); BAR(); STAMP(20); }
      if (kk == 26) { VMWAIT(10); BAR(); }
      if (kk < 10) DMA_B(kk);
      if (kk > 20) { LDW4(2 * k - 2); LDW4(2 * k - 1); }
      CFENCE();
      if (kk == 15 || kk == 31) { lb += 65536; asm volatile("" : "+v"(lb)); }
      if (kk < 39) {
#pragma unroll
        for (int r = 0; r < 4; ++r)
          af[(kk + 1) & 1][r] = kk == 38 && r == 3 ? *(lds_u32x4*)(smem + lane16) : *(lds_u32x4*)(lb + (((kk + 1) * 4 + r) & 63) * 1024);
      }
#pragma unroll
      for (int r = 0; r < 4; ++r) {
        ag[h4 + r] = MFMA(w3[0][k], af[kk & 1][r], k == 0 ? zero4 : ag[h4 + r]);
        au[h4 + r] = MFMA(w3[1][k], af[kk & 1][r], k == 0 ? zero4 : au[h4 + r]);
      }
      if (kk >= 5 && kk < 25 - W3PRE) { cg = MFMA(w3[0][(kk + W3PRE - 5) % 20], ones, cg); cu = MFMA(w3[1][(kk + W3PRE - 5) % 20], ones, cu); }
#pragma unroll
      for (int r = 0; r < 4; ++r) { __builtin_amdgcn_sched_group_barrier(0x100, 1, 0); __builtin_amdgcn_sched_group_barrier(0x8, 2, 0); }
      CFENCE();
     }
     if (h4) { STAMP(21); f2 = __builtin_amdgcn_raw_buffer_load_b64(rs, li * 64, WS_FLAG + (2 * 256 + g * 16) * 64, 16); CFENCE(); }
#pragma unroll
     for (int r = 0; r < 4; ++r) { epi(h4 + r); if (r & 1) CFENCE(); }
    };
    halfbody(std::integral_constant<int, 0>{});
    halfbody(std::integral_constant<int, 1>{});
    __builtin_amdgcn_s_setprio(0);
  }
  {
    auto gemm = [&](int half) {
#pragma unroll
      for (int r = 0; r < 2; ++r) {
        f32x4 acc[5];
#pragma unroll
        for (int k = 0; k < 6; ++k)
#pragma unroll
          for (int t = 0; t < 5; ++t) acc[t] = MFMA(w4[k * 5 + t], a[r][k], k == 0 ? zero4 : acc[t]);
        if (w < 6) {
#pragma unroll
          for (int t = 0; t < 5; ++t) acc[t] = MFMA(w4[30 + t], a[r][6], acc[t]);
        }
#pragma unroll
        for (int t = 0; t < 5; ++t) *(lds_f32x4*)(smem + (((half * 2 + r) * 5 + t) * 8 + w) * 1024 + lane16) = acc[t];
      }
      CFENCE();
    };
    auto fin = [&](int n) {
      const int r = un[n] / 5, t = un[n] - 5 * r;
      f32x4 s = {bf_lo(res[n][0]), bf_hi(res[n][0]), bf_lo(res[n][1]), bf_hi(res[n][1])};
#pragma unroll
      for (int i = 0; i < 8; ++i) s += *(lds_f32x4*)(smem + (un[n] * 8 + i) * 1024 + lane16);
      int l2 = lane16;
      asm volatile("" : "+v"(l2));
      *(u32x2*)(out + ((rtb + (r >> 1) * 4 + (r & 1)) * 16 + ((l2 >> 4) & 15)) * D + 80 * p + 16 * t + 4 * (l2 >> 8)) =
          u32x2{pk_bf16(s[0], s[1]), pk_bf16(s[2], s[3])};
    };
    CFENCE();
    if (w == 7 || !__all(f2[0] == (unsigned)tag && f2[1] == (unsigned)(tag >> 32))) wait_group(ws, 2, g, lane, tag);
    CFENCE();
    lda(0);
#pragma unroll
    for (int n = 0; n < 3; ++n) {
      const int r = un[n] / 5, t = un[n] - 5 * r;
      res[n] = __builtin_amdgcn_raw_buffer_load_b64(rs, cst, WS_H + (rtb + (r >> 1) * 4 + (r & 1)) * 20 * 1024 + (10 * p + 2 * t) * 256, 16);
    }
    CFENCE();
    VMWAIT(17);
    BAR();
    raise(3);
    STAMP(8);
    gemm(0);
    if (w == 7) wait_group(ws, 3, g, lane, tag);
    BAR();
    STAMP(9);
    if (b == 0 && tid == 0) __hip_atomic_store((gu64*)(ws + WS_EPOCH), tag, RLX_AGENT);
    lda(1);
    fin(0);
    if (w < 2) fin(2);
    CFENCE();
    gemm(1);
    STAMP(22);
    BAR();
    fin(1);
    if (w == 2 || w == 3) fin(2);
  }
  STAMP(10); STAMPC(31);
}

extern "C" void kernel_launch(void* const* d_in, const int* in_sizes, int n_in, void* d_out, int out_size,
                              void* d_ws, size_t ws_size, hipStream_t stream) {
  fused_block<<<256, 512, 0, stream>>>((const unsigned short*)d_in[0], (const unsigned short*)d_in[1],
                                       (const unsigned short*)d_in[2], (const unsigned short*)d_in[4],
                                       (const unsigned short*)d_in[5], (const unsigned short*)d_in[6],
                                       (unsigned short*)d_out, (char*)d_ws);
}
```
